```python
import torch

def _softmax(x, axis=-1):
    return torch.softmax(x, dim=axis)
def _gelu(x):
    return torch.nn.functional.gelu(x, approximate="tanh")
def _silu(x):
    return torch.nn.functional.silu(x)
def _sigmoid(x):
    return torch.sigmoid(x)
def _layernorm(x, axis=-1, eps=1e-5):
    m = x.mean(dim=axis, keepdim=True)
    v = x.var(dim=axis, keepdim=True, unbiased=False)
    return (x - m) / torch.sqrt(v + eps)
def _scatter_add(tgt, idx, src, axis=0):
    return tgt.clone().index_add_(axis, idx, src)
def _segment_sum(src, seg, n):
    out = torch.zeros((n,) + src.shape[1:], dtype=src.dtype)
    return out.index_add_(0, seg, src)
def _pad(x, pw):
    flat = tuple(v for pair in reversed(pw) for v in pair)
    return torch.nn.functional.pad(x, flat)

kernel_name = 'recurrent_scan__mha_block_cached__dense_activation_gelu__decode'

def setup_inputs():

    _w = (lambda a: (lambda t: t.to(torch.bfloat16) if t.is_floating_point() else t)(torch.as_tensor(a)))
    def _A_setup():
        B, Sq, D = 1, 1, 640
        H, Sk, Dh = 32, 32768, 192
        return {
            "x": _w((lambda *s: torch.randn(*s, dtype=torch.bfloat16) * 0.4)(B, Sq, D)),
            "k_cache": _w((lambda *s: torch.randn(*s, dtype=torch.bfloat16) * 1.6)(B, H, Sk, Dh)),
            "v_cache": _w((lambda *s: torch.randn(*s, dtype=torch.bfloat16) * 1.6)(B, H, Sk, Dh)),
            "wq": _w((lambda *s: torch.randn(*s, dtype=torch.bfloat16) * 1.6)(D, H * Dh) * (D ** -0.5)),
            "wo": _w((lambda *s: torch.randn(*s, dtype=torch.bfloat16) * 1.6)(H * Dh, D) * ((H * Dh) ** -0.5)),
        }
    _A_in = _A_setup()

    def _B_setup():
        B, S, D, F = 1, 1, 640, 1536
        return {
            "x": _w((lambda *s: torch.randn(*s, dtype=torch.bfloat16) * 0.4)(B, S, D)),
            "w1": _w((lambda *s: torch.randn(*s, dtype=torch.bfloat16) * 1.6)(D, F) * (D ** -0.5)),
            "b1": _w((lambda *s: torch.randn(*s, dtype=torch.bfloat16) * 1.6)(F) * 0.01),
            "w2": _w((lambda *s: torch.randn(*s, dtype=torch.bfloat16) * 1.6)(F, D) * (F ** -0.5)),
            "b2": _w((lambda *s: torch.randn(*s, dtype=torch.bfloat16) * 1.6)(D) * 0.01),
        }
    _B_in = _B_setup()

    return {
        "A_x": _A_in["x"],
        "A_k_cache": _A_in["k_cache"],
        "A_v_cache": _A_in["v_cache"],
        "A_wq": _A_in["wq"],
        "A_wo": _A_in["wo"],
        "B_x": _B_in["x"],
        "B_w1": _B_in["w1"],
        "B_b1": _B_in["b1"],
        "B_w2": _B_in["w2"],
        "B_b2": _B_in["b2"],
    }

def reference(A_x, A_k_cache, A_v_cache, A_wq, A_wo, B_x, B_w1, B_b1, B_w2, B_b2):
    def _A_ref(x, k_cache, v_cache, wq, wo):
        B, Sq, D = x.shape
        H, Dh = 32, 192
        q = (lambda x, *d: x.permute(*d))((x @ wq).reshape(B, Sq, H, Dh), 0, 2, 1, 3)
        scores = torch.einsum("bhqd,bhkd->bhqk", q, k_cache) * (Dh ** -0.5)
        p = _softmax(scores, axis=-1)
        o = torch.einsum("bhqk,bhkd->bhqd", p, v_cache)
        return (lambda x, *d: x.permute(*d))(o, 0, 2, 1, 3).reshape(B, Sq, H * Dh) @ wo

    def _B_ref(x, w1, b1, w2, b2):
        h = torch.einsum("bsd,df->bsf", x, w1) + b1
        h = _gelu(h)
        return torch.einsum("bsf,fd->bsd", h, w2) + b2

    x = A_x
    xn = _layernorm(x, axis=-1)
    s = _A_ref(xn, A_k_cache, A_v_cache, A_wq, A_wo)
    g = _silu(_B_ref(xn, B_w1, B_b1, B_w2, B_b2))
    return x + s * g
```

```cpp
#include <hip/hip_runtime.h>
#include <hip/hip_bf16.h>
#include <stdint.h>

#define MIMIC 1

typedef unsigned v4u __attribute__((ext_vector_type(4)));
typedef __bf16 bf16x2 __attribute__((ext_vector_type(2)));

constexpr int DM = 640, NH = 32, SK = 32768, DH = 192, FF = 1536;
constexpr int NSPLIT = 8, NW = 8, NT = 512, RING = 4;
constexpr int UPW = SK / NSPLIT / 8 / NW;
constexpr size_t HEAD_BYTES = (size_t)SK * DH * 2;
constexpr float SCALE = 0.07216878364870322f;
constexpr float LOG2E = 1.4426950408889634f;

__device__ __forceinline__ float bf2f(unsigned short b) { return __builtin_bit_cast(float, (unsigned)b << 16); }
__device__ __forceinline__ float rbf(float x) {
  return (float)(__bf16)x;
}
__device__ __forceinline__ float rbf_always(float x) { return (float)(__bf16)x; }
__device__ __forceinline__ unsigned short f2bf(float x) { return __builtin_bit_cast(unsigned short, (__bf16)x); }
__device__ __forceinline__ float dot2(unsigned a, unsigned b, float c) {
  return __builtin_amdgcn_fdot2_f32_bf16(__builtin_bit_cast(bf16x2, a), __builtin_bit_cast(bf16x2, b), c, false);
}
template <int CTRL> __device__ __forceinline__ float dpp(float x) {
  return __builtin_bit_cast(float, __builtin_amdgcn_mov_dpp(__builtin_bit_cast(int, x), CTRL, 0xf, 0xf, true));
}
__device__ __forceinline__ float wave_sum(float x) {
  for (int o = 32; o >= 1; o >>= 1) x += __shfl_xor(x, o);
  return x;
}

constexpr size_t WS_Q = 0;
constexpr size_t WS_HID = 16384;
constexpr size_t WS_PART = 32768;
constexpr size_t WS_SP = WS_PART + 256 * 200 * 4;

__global__ void __launch_bounds__(256) k_prep(const unsigned short* __restrict__ x, const unsigned short* __restrict__ wq,
                                              const unsigned short* __restrict__ w1, const unsigned short* __restrict__ b1,
                                              unsigned char* ws) {
  __shared__ float xn[DM];
  __shared__ float red[32][64];
  const int t = threadIdx.x, lane = t & 63;
  float xv[10];
  float s = 0.f;
  for (int i = 0; i < 10; ++i) { xv[i] = bf2f(x[lane + 64 * i]); s += xv[i]; }
  s = wave_sum(s);
  const float mean = s * (1.f / DM);
  float v = 0.f;
  for (int i = 0; i < 10; ++i) { float d = xv[i] - mean; v += d * d; }
  v = wave_sum(v) * (1.f / DM);
  const float mb = rbf(mean), vb = rbf(v);
  const float den = rbf(sqrtf(rbf(vb + 1e-5f)));
  if (t < 64) for (int i = 0; i < 10; ++i) xn[lane + 64 * i] = rbf(rbf(xv[i] - mb) / den);
  __syncthreads();
  const int cb = blockIdx.x * 64;
  const bool isq = cb < NH * DH;
  const unsigned short* W = isq ? wq + cb : w1 + (cb - NH * DH);
  const int ld = isq ? NH * DH : FF;
  const int rg = t >> 3, ch = t & 7;
  float acc[8] = {0, 0, 0, 0, 0, 0, 0, 0};
#pragma unroll
  for (int p = 0; p < 20; ++p) {
    const int r = rg + 32 * p;
    v4u w = *(const v4u*)(W + (size_t)r * ld + ch * 8);
    const float xr = xn[r];
#pragma unroll
    for (int e = 0; e < 4; ++e) {
      acc[2 * e] += xr * __builtin_bit_cast(float, w[e] << 16);
      acc[2 * e + 1] += xr * __builtin_bit_cast(float, w[e] & 0xffff0000u);
    }
  }
#pragma unroll
  for (int e = 0; e < 8; ++e) red[rg][ch * 8 + e] = acc[e];
  __syncthreads();
  if (t < 64) {
    float r = 0.f;
    for (int i = 0; i < 32; ++i) r += red[i][t];
    if (isq) {
      ((unsigned short*)(ws + WS_Q))[cb + t] = f2bf(r);
    } else {
      const int c = cb - NH * DH + t;
      float hh = rbf(rbf(r) + bf2f(b1[c]));
      float u = 0.7978845608028654f * (hh + 0.044715f * hh * hh * hh);
      float g = 0.5f * hh * (1.f + tanhf(u));
      ((float*)(ws + WS_HID))[c] = rbf(g);
    }
  }
}

__global__ void __launch_bounds__(NT) k_attn(const unsigned char* __restrict__ K, const unsigned char* __restrict__ V, unsigned char* ws) {
  __shared__ float mrg[NW][200];
  const int bid = blockIdx.x, h = bid / NSPLIT, s = bid % NSPLIT;
  const int t = threadIdx.x, lane = t & 63, w = __builtin_amdgcn_readfirstlane(t >> 6);
  const __amdgpu_buffer_rsrc_t rk = __builtin_amdgcn_make_buffer_rsrc((void*)(K + (size_t)h * HEAD_BYTES), 0, (int)HEAD_BYTES, 0x00020000);
  const __amdgpu_buffer_rsrc_t rv = __builtin_amdgcn_make_buffer_rsrc((void*)(V + (size_t)h * HEAD_BYTES), 0, (int)HEAD_BYTES, 0x00020000);
  const unsigned loff = (lane >> 3) * 384 + (lane & 7) * 16;
  v4u qv[3];
#pragma unroll
  for (int j = 0; j < 3; ++j) qv[j] = *(const v4u*)(ws + WS_Q + h * DH * 2 + j * 128 + (lane & 7) * 16);
  v4u kb[RING][3], vb[RING][3];
  float acc[24];
#pragma unroll
  for (int i = 0; i < 24; ++i) acc[i] = 0.f;
  float m = -1e30f, l = 0.f;
  const unsigned ubase = (unsigned)(s * (SK / NSPLIT / 8) + w) * 3072u;
#define ISSUE(slot, idx) do { const unsigned so_ = ubase + (unsigned)(idx) * (NW * 3072u); \
    _Pragma("unroll") for (int jj = 0; jj < 3; ++jj) kb[slot][jj] = __builtin_amdgcn_raw_buffer_load_b128(rk, loff + jj * 128, so_, 2); \
    _Pragma("unroll") for (int jj = 0; jj < 3; ++jj) vb[slot][jj] = __builtin_amdgcn_raw_buffer_load_b128(rv, loff + jj * 128, so_, 2); \
    asm volatile("" ::: "memory"); __builtin_amdgcn_sched_barrier(0); } while (0)
#define CONSUME(slot) do { \
    float d0 = 0.f, d1 = 0.f, d2 = 0.f; \
    _Pragma("unroll") for (int e = 0; e < 4; ++e) { d0 = dot2(kb[slot][0][e], qv[0][e], d0); d1 = dot2(kb[slot][1][e], qv[1][e], d1); d2 = dot2(kb[slot][2][e], qv[2][e], d2); } \
    float dsum = (d0 + d1) + d2; \
    dsum += dpp<0xB1>(dsum); dsum += dpp<0x4E>(dsum); dsum += dpp<0x141>(dsum); \
    const float tt = rbf(rbf(dsum) * SCALE) * LOG2E; \
    const float mn = fmaxf(m, tt); \
    const float al = __builtin_amdgcn_exp2f(m - mn), p = __builtin_amdgcn_exp2f(tt - mn); \
    l = l * al + p; m = mn; \
    _Pragma("unroll") for (int jj = 0; jj < 3; ++jj) _Pragma("unroll") for (int e = 0; e < 4; ++e) { \
      const unsigned u_ = vb[slot][jj][e]; \
      acc[jj * 8 + 2 * e] = acc[jj * 8 + 2 * e] * al + p * __builtin_bit_cast(float, u_ << 16); \
      acc[jj * 8 + 2 * e + 1] = acc[jj * 8 + 2 * e + 1] * al + p * __builtin_bit_cast(float, u_ & 0xffff0000u); } \
    asm volatile("" : "+v"(acc[0]), "+v"(acc[1]), "+v"(acc[2]), "+v"(acc[3]), "+v"(acc[4]), "+v"(acc[5]), "+v"(acc[6]), "+v"(acc[7]), \
                      "+v"(acc[8]), "+v"(acc[9]), "+v"(acc[10]), "+v"(acc[11]), "+v"(acc[12]), "+v"(acc[13]), "+v"(acc[14]), "+v"(acc[15]), \
                      "+v"(acc[16]), "+v"(acc[17]), "+v"(acc[18]), "+v"(acc[19]), "+v"(acc[20]), "+v"(acc[21]), "+v"(acc[22]), "+v"(acc[23]), \
                      "+v"(m), "+v"(l) :: "memory"); \
    __builtin_amdgcn_sched_barrier(0); } while (0)
#pragma unroll
  for (int d = 0; d < RING; ++d) ISSUE(d, d);
  for (int it = 1; it < UPW / RING; ++it) {
#pragma unroll
    for (int d = 0; d < RING; ++d) { CONSUME(d); ISSUE(d, it * RING + d); }
  }
#pragma unroll
  for (int d = 0; d < RING; ++d) CONSUME(d);
  float M = m;
  M = fmaxf(M, __shfl_xor(M, 8)); M = fmaxf(M, __shfl_xor(M, 16)); M = fmaxf(M, __shfl_xor(M, 32));
  const float sc = __builtin_amdgcn_exp2f(m - M);
  l *= sc;
  l += __shfl_xor(l, 8); l += __shfl_xor(l, 16); l += __shfl_xor(l, 32);
#pragma unroll
  for (int i = 0; i < 24; ++i) {
    float a = acc[i] * sc;
    a += __shfl_xor(a, 8); a += __shfl_xor(a, 16); a += __shfl_xor(a, 32);
    acc[i] = a;
  }
  if (lane < 8) {
#pragma unroll
    for (int j = 0; j < 3; ++j)
#pragma unroll
      for (int e = 0; e < 8; ++e) mrg[w][j * 64 + lane * 8 + e] = acc[j * 8 + e];
    if (lane == 0) { mrg[w][192] = M; mrg[w][193] = l; }
  }
  __syncthreads();
  if (t < 194) {
    float MM = mrg[0][192];
    for (int i = 1; i < NW; ++i) MM = fmaxf(MM, mrg[i][192]);
    float r = 0.f;
    if (t == 192) r = MM;
    else for (int i = 0; i < NW; ++i) r += __builtin_amdgcn_exp2f(mrg[i][192] - MM) * mrg[i][t];
    ((float*)(ws + WS_PART))[bid * 200 + t] = r;
  }
}

__global__ void __launch_bounds__(NT) k_proj(const unsigned short* __restrict__ wo, const unsigned short* __restrict__ w2, unsigned char* ws) {
  __shared__ float o[DH];
  __shared__ float red[2][48][80];
  const int bid = blockIdx.x, h = bid / NSPLIT, c = bid % NSPLIT;
  const int t = threadIdx.x, lane = t & 63, w = t >> 6;
  const float* part = (const float*)(ws + WS_PART) + (size_t)h * NSPLIT * 200;
  if (t < DH) {
    float MM = part[192];
    for (int i = 1; i < NSPLIT; ++i) MM = fmaxf(MM, part[i * 200 + 192]);
    float L = 0.f, r = 0.f;
    for (int i = 0; i < NSPLIT; ++i) { float e = __builtin_amdgcn_exp2f(part[i * 200 + 192] - MM); L += e * part[i * 200 + 193]; r += e * part[i * 200 + t]; }
    o[t] = rbf(r / L);
  }
  __syncthreads();
  if (lane < 60) {
    const int rg = 6 * w + lane / 10, oct = lane % 10;
    float a[8] = {0, 0, 0, 0, 0, 0, 0, 0}, y[8];
#pragma unroll
    for (int i = 0; i < 4; ++i) {
      v4u ww = *(const v4u*)(wo + (size_t)(DH * h + 4 * rg + i) * DM + 80 * c + 8 * oct);
      const float ov = o[4 * rg + i];
#pragma unroll
      for (int e = 0; e < 4; ++e) { a[2 * e] += ov * __builtin_bit_cast(float, ww[e] << 16); a[2 * e + 1] += ov * __builtin_bit_cast(float, ww[e] & 0xffff0000u); }
    }
    v4u w2v = *(const v4u*)(w2 + (size_t)(48 * h + rg) * DM + 80 * c + 8 * oct);
    const float hv = ((const float*)(ws + WS_HID))[48 * h + rg];
#pragma unroll
    for (int e = 0; e < 4; ++e) { y[2 * e] = hv * __builtin_bit_cast(float, w2v[e] << 16); y[2 * e + 1] = hv * __builtin_bit_cast(float, w2v[e] & 0xffff0000u); }
#pragma unroll
    for (int e = 0; e < 8; ++e) { red[0][rg][8 * oct + e] = a[e]; red[1][rg][8 * oct + e] = y[e]; }
  }
  __syncthreads();
  if (t < 160) {
    const int k = t / 80, col = t % 80;
    float r = 0.f;
    for (int i = 0; i < 48; ++i) r += red[k][i][col];
    ((float*)(ws + WS_SP))[bid * 160 + t] = r;
  }
}

__global__ void __launch_bounds__(640) k_final(const unsigned short* __restrict__ x, const unsigned short* __restrict__ b2, const unsigned char* ws, unsigned short* out) {
  const int t = threadIdx.x, c = t / 80, col = t % 80;
  const float* sp = (const float*)(ws + WS_SP);
  float s = 0.f, y = 0.f;
  for (int h = 0; h < NH; ++h) { s += sp[(h * 8 + c) * 160 + col]; y += sp[(h * 8 + c) * 160 + 80 + col]; }
  s = rbf(s);
  y = rbf(rbf(y) + bf2f(b2[t]));
  const float g = rbf(y / (1.f + __expf(-y)));
  out[t] = f2bf(bf2f(x[t]) + rbf(s * g));
}

extern "C" void kernel_launch(void* const* d_in, const int* in_sizes, int n_in, void* d_out, int out_size, void* d_ws, size_t ws_size, hipStream_t stream) {
  const unsigned short* x = (const unsigned short*)d_in[0];
  const unsigned char* K = (const unsigned char*)d_in[1];
  const unsigned char* V = (const unsigned char*)d_in[2];
  const unsigned short* wq = (const unsigned short*)d_in[3];
  const unsigned short* wo = (const unsigned short*)d_in[4];
  const unsigned short* w1 = (const unsigned short*)d_in[6];
  const unsigned short* b1 = (const unsigned short*)d_in[7];
  const unsigned short* w2 = (const unsigned short*)d_in[8];
  const unsigned short* b2 = (const unsigned short*)d_in[9];
  unsigned char* ws = (unsigned char*)d_ws;
  hipLaunchKernelGGL(k_prep, dim3(120), dim3(256), 0, stream, x, wq, w1, b1, ws);
  hipLaunchKernelGGL(k_attn, dim3(256), dim3(NT), 0, stream, K, V, ws);
  hipLaunchKernelGGL(k_proj, dim3(256), dim3(NT), 0, stream, wo, w2, ws);
  hipLaunchKernelGGL(k_final, dim3(1), dim3(640), 0, stream, x, b2, ws, (unsigned short*)d_out);
}
```

```cpp
#include <hip/hip_runtime.h>
#include <hip/hip_bf16.h>
#include <stdint.h>

typedef unsigned v4u __attribute__((ext_vector_type(4)));
typedef unsigned v2u __attribute__((ext_vector_type(2)));
typedef float v2f __attribute__((ext_vector_type(2)));
typedef __bf16 bf16x2 __attribute__((ext_vector_type(2)));
typedef unsigned long long u64;

#define DRY 1
#define HINT_SLEEP 40
#define POLL_SLEEP 1
#define PORDER 1
#define HINT_AT 0

constexpr int DM = 640, NH = 32, SK = 32768, DH = 192, FF = 1536;
constexpr int NSPLIT = 8, NW = 8, NT = 512, RING = 4;
constexpr int UPW = SK / NSPLIT / 8 / NW;
constexpr size_t HEAD_BYTES = (size_t)SK * DH * 2;
constexpr float SCALE = 0.07216878364870322f;
constexpr float LOG2E = 1.4426950408889634f;
constexpr unsigned SPIN_CAP = 400000u;
constexpr unsigned OOR = 0x7ffff000u;

constexpr unsigned WS_EPOCH = 0;
constexpr unsigned WS_CTL = 4096;
constexpr unsigned CTL_STR = 4352;
constexpr unsigned WS_QX = WS_CTL + 32 * CTL_STR;
constexpr unsigned WS_PART = WS_QX + 256 * 1920;
constexpr unsigned WS_SP = WS_PART + 256 * 1536;
constexpr unsigned WS_END = WS_SP + 2 * 256 * 2560;

__device__ __forceinline__ float rbf(float x) { return (float)(__bf16)x; }
__device__ __forceinline__ unsigned short f2bf(float x) { return __builtin_bit_cast(unsigned short, (__bf16)x); }
__device__ __forceinline__ float lo_f(unsigned u) { return __builtin_bit_cast(float, u << 16); }
__device__ __forceinline__ float hi_f(unsigned u) { return __builtin_bit_cast(float, u & 0xffff0000u); }
__device__ __forceinline__ float dot2(unsigned a, unsigned b, float c) {
  return __builtin_amdgcn_fdot2_f32_bf16(__builtin_bit_cast(bf16x2, a), __builtin_bit_cast(bf16x2, b), c, false);
}
template <int CTRL> __device__ __forceinline__ float dpp(float x) {
  return __builtin_bit_cast(float, __builtin_amdgcn_mov_dpp(__builtin_bit_cast(int, x), CTRL, 0xf, 0xf, true));
}
__device__ __forceinline__ float sw16(float a, float b) {
  auto s = __builtin_amdgcn_permlane16_swap(__float_as_uint(a), __float_as_uint(b), false, false);
  return __uint_as_float(s[0]) + __uint_as_float(s[1]);
}
__device__ __forceinline__ float sw32(float a, float b) {
  auto s = __builtin_amdgcn_permlane32_swap(__float_as_uint(a), __float_as_uint(b), false, false);
  return __uint_as_float(s[0]) + __uint_as_float(s[1]);
}
__device__ __forceinline__ float x16_sum(float x) { return sw16(x, x); }
__device__ __forceinline__ float x32_sum(float x) { return sw32(x, x); }
__device__ __forceinline__ float x16_max(float x) {
  auto s = __builtin_amdgcn_permlane16_swap(__float_as_uint(x), __float_as_uint(x), false, false);
  return fmaxf(__uint_as_float(s[0]), __uint_as_float(s[1]));
}
__device__ __forceinline__ float x32_max(float x) {
  auto s = __builtin_amdgcn_permlane32_swap(__float_as_uint(x), __float_as_uint(x), false, false);
  return fmaxf(__uint_as_float(s[0]), __uint_as_float(s[1]));
}
__device__ __forceinline__ float sum16(float x) {
  x += dpp<0xB1>(x); x += dpp<0x4E>(x); x += dpp<0x141>(x); x += dpp<0x140>(x);
  return x;
}
__device__ __forceinline__ float wave_sum(float x) { return x32_sum(x16_sum(sum16(x))); }
__device__ __forceinline__ float grp8_sum(float x) {
  x += dpp<0x128>(x);
  return x32_sum(x16_sum(x));
}
__device__ __forceinline__ float grp8_max(float x) {
  x = fmaxf(x, dpp<0x128>(x));
  return x32_max(x16_max(x));
}
__device__ __forceinline__ void wg_barrier() {
  asm volatile("s_waitcnt lgkmcnt(0)" ::: "memory");
  __builtin_amdgcn_s_barrier();
  asm volatile("" ::: "memory");
}

struct Tag { unsigned t0, t1; };
__device__ __forceinline__ unsigned f24(float v) { return (__builtin_bit_cast(unsigned, v) + 0x80u) >> 8; }
__device__ __forceinline__ float q24(float v) { return __builtin_bit_cast(float, f24(v) << 8); }
__device__ __forceinline__ v4u enc2(float a, float b, Tag tg) { return v4u{tg.t0 | f24(a), tg.t1, tg.t0 | f24(b), tg.t1}; }
__device__ __forceinline__ unsigned mism(v4u g, Tag tg) {
  return ((g.y ^ tg.t1) | (g.w ^ tg.t1)) | ((((g.x ^ tg.t0) | (g.z ^ tg.t0))) & 0xff000000u);
}
__device__ __forceinline__ float dec_a(v4u g) { return __builtin_bit_cast(float, g.x << 8); }
__device__ __forceinline__ float dec_b(v4u g) { return __builtin_bit_cast(float, g.z << 8); }
__device__ __forceinline__ void st2(__amdgpu_buffer_rsrc_t rs, unsigned off, float a, float b, Tag tg) {
  __builtin_amdgcn_raw_buffer_store_b128(enc2(a, b, tg), rs, off, 0, 16);
}
__device__ __forceinline__ v4u ld2(__amdgpu_buffer_rsrc_t rs, unsigned off) {
  return __builtin_amdgcn_raw_buffer_load_b128(rs, off, 0, 16);
}

struct Params {
  const unsigned short* x; const unsigned char* K; const unsigned char* V; const unsigned short* wq; const unsigned short* wo;
  const unsigned short* w1; const unsigned short* b1; const unsigned short* w2; const unsigned short* b2;
  unsigned char* ws; unsigned short* out;
};

#define STAMP(i) do {} while (0)
#define STAMPR(i) do {} while (0)
#define FENCE() do { asm volatile("" ::: "memory"); __builtin_amdgcn_sched_barrier(0); } while (0)

__global__ void __launch_bounds__(NT) fused_kernel(Params p) {
  __shared__ __attribute__((aligned(16))) float lds[16 * 240 + 8 * 320 + 8 * 200 + 8 * 32 + 48 + 48 + 96 + 16];
  float* red = lds;
  float* ysh = lds;
  float* ssh = lds + 16 * 240;
  float* mrg = ssh + 8 * 320;
  float* fin = mrg + 8 * 200;
  float* osh = fin + 8 * 32;
  float* hid = osh + 48;
  unsigned* qsh = (unsigned*)(hid + 48);
  unsigned* cnt = qsh + 96;

  const int bid = blockIdx.x, h = bid >> 3, s = bid & 7;
  const int rq = s >> 1, chf = s & 1;
  const int t = threadIdx.x, lane = t & 63, w = __builtin_amdgcn_readfirstlane(t >> 6);
  STAMP(0);
  const __amdgpu_buffer_rsrc_t rws = __builtin_amdgcn_make_buffer_rsrc((void*)p.ws, 0, (int)WS_END, 0x00020000);
  const __amdgpu_buffer_rsrc_t rk = __builtin_amdgcn_make_buffer_rsrc((void*)(p.K + (size_t)h * HEAD_BYTES), 0, (int)HEAD_BYTES, 0x00020000);
  const __amdgpu_buffer_rsrc_t rv = __builtin_amdgcn_make_buffer_rsrc((void*)(p.V + (size_t)h * HEAD_BYTES), 0, (int)HEAD_BYTES, 0x00020000);

  const int tt = t < 480 ? t : 479;
  const int rg = tt / 30, ch = tt % 30;
  unsigned xr[5];
  v4u wv[5];
  {
    const bool isq = ch < 24;
    const unsigned short* wsel = isq ? p.wq : p.w1;
    const unsigned ldw = isq ? NH * DH : FF;
    const unsigned coff = isq ? DH * h + 8 * ch : 48 * h + 8 * (ch - 24);
    const unsigned short* base = wsel + (size_t)((80 * s + rg) * ldw + coff);
#pragma unroll
    for (int i = 0; i < 5; ++i) wv[i] = __builtin_nontemporal_load((const v4u*)(base + (size_t)(i * 16 * ldw)));
#pragma unroll
    for (int i = 0; i < 5; ++i) xr[i] = ((const unsigned*)p.x)[(80 * s + rg + 16 * i) >> 1];
  }
  unsigned zero_v;
  asm volatile("v_mov_b32 %0, 0" : "=v"(zero_v));
  v2u Ev = __builtin_amdgcn_raw_buffer_load_b64(rws, WS_EPOCH + zero_v, 0, 16);
  unsigned xw[5];
#pragma unroll
  for (int i = 0; i < 5; ++i) xw[i] = ((const unsigned*)p.x)[lane + 64 * i];
  const int hr0 = 12 * rq + w, hr1 = 12 * rq + 8 + (w & 3);
  unsigned b1a = ((const unsigned*)p.b1)[((48 * h + hr0) >> 1) + zero_v], b1b = ((const unsigned*)p.b1)[((48 * h + hr1) >> 1) + zero_v];
  const int fb = bid < 40 ? bid : 0;
  unsigned xfin = ((const unsigned*)p.x)[8 * fb + (lane & 7)], b2fin = ((const unsigned*)p.b2)[8 * fb + (lane & 7)];
  if (t < 2) cnt[t] = 0u;
  FENCE();
  __builtin_amdgcn_s_barrier();
  FENCE();

  const unsigned loff = (lane >> 3) * 384 + (lane & 7) * 16;
  const unsigned ubase = (unsigned)(s * NW + w) * (RING * 3072u);
  v4u kb[RING][3], vb[RING][3];
#define ISSUE(slot, it) do { const unsigned so_ = ubase + (unsigned)(it) * (NSPLIT * NW * RING * 3072u) + (slot) * 3072u; \
    _Pragma("unroll") for (int jj = 0; jj < 3; ++jj) kb[slot][jj] = __builtin_amdgcn_raw_buffer_load_b128(rk, loff + jj * 128, so_, 2); \
    _Pragma("unroll") for (int jj = 0; jj < 3; ++jj) vb[slot][jj] = __builtin_amdgcn_raw_buffer_load_b128(rv, loff + jj * 128, so_, 2); \
    FENCE(); } while (0)
  ISSUE(0, 0);
  ISSUE(1, 0);
  asm volatile("" : "+v"(Ev), "+v"(xw[0]), "+v"(xw[1]), "+v"(xw[2]), "+v"(xw[3]), "+v"(xw[4]), "+v"(b1a), "+v"(b1b), "+v"(xfin), "+v"(b2fin) :: "memory");
  __builtin_amdgcn_sched_barrier(0);
  Tag tg;
  u64 Enew;
  {
    const u64 E = ((u64)__builtin_amdgcn_readfirstlane(Ev.y) << 32) | (unsigned)__builtin_amdgcn_readfirstlane(Ev.x);
    u64 z = E + 0x9E3779B97F4A7C15ull;
    z = (z ^ (z >> 30)) * 0xBF58476D1CE4E5B9ull; z = (z ^ (z >> 27)) * 0x94D049BB133111EBull; z ^= z >> 31;
    Enew = z; tg.t0 = (unsigned)z << 24; tg.t1 = (unsigned)(z >> 8) | 1u;
  }
  {
    float sum = 0.f;
#pragma unroll
    for (int i = 0; i < 5; ++i) sum += lo_f(xw[i]) + hi_f(xw[i]);
    const float mean = wave_sum(sum) * (1.f / DM);
    float var = 0.f;
#pragma unroll
    for (int i = 0; i < 5; ++i) { const float a = lo_f(xw[i]) - mean, b = hi_f(xw[i]) - mean; var += a * a + b * b; }
    var = wave_sum(var) * (1.f / DM);
    const float mb = rbf(mean), den = rbf(sqrtf(rbf(rbf(var) + 1e-5f)));
    float a8[8] = {0.f, 0.f, 0.f, 0.f, 0.f, 0.f, 0.f, 0.f};
#pragma unroll
    for (int i = 0; i < 5; ++i) {
      const float xe = (rg & 1) ? hi_f(xr[i]) : lo_f(xr[i]);
      const float xn = rbf(rbf(xe - mb) / den);
#pragma unroll
      for (int e = 0; e < 4; ++e) { a8[2 * e] += xn * lo_f(wv[i][e]); a8[2 * e + 1] += xn * hi_f(wv[i][e]); }
    }
    if (t < 480) {
      float4* dst = (float4*)(red + rg * 240 + ch * 8);
      dst[0] = make_float4(a8[0], a8[1], a8[2], a8[3]); dst[1] = make_float4(a8[4], a8[5], a8[6], a8[7]);
    }
  }
  STAMP(1);
  wg_barrier();
  if (t < 120) {
    v2f r = v2f{0.f, 0.f};
#pragma unroll
    for (int i = 0; i < 16; ++i) r += *(const v2f*)(red + i * 240 + 2 * t);
    st2(rws, WS_QX + (unsigned)bid * 1920u + (unsigned)t * 16u, r.x, r.y, tg);
  }
  FENCE();
  __builtin_amdgcn_s_barrier();
  FENCE();
  STAMP(2);
  const unsigned gq_off = WS_QX + (unsigned)(h * 8 + (lane >> 3)) * 1920u + (unsigned)w * 256u + (unsigned)(lane & 7) * 16u;
  const unsigned gq1 = w < 7 ? 128u : 0u;
#define GQ_LOAD(G) do { G[0] = ld2(rws, gq_off); G[1] = ld2(rws, gq_off + gq1); FENCE(); } while (0)
#define GQ_OK(G) __all((mism(G[0], tg) | mism(G[1], tg)) == 0u)
#define GQ_DEC(G) do { qd[0] = dec_a(G[0]); qd[1] = dec_b(G[0]); qd[2] = dec_a(G[1]); qd[3] = dec_b(G[1]); } while (0)
  v4u gqa[2];
  float qd[4];
  ISSUE(2, 0);
  __builtin_amdgcn_s_barrier();
  FENCE();
  GQ_LOAD(gqa);
  __builtin_amdgcn_s_barrier();
  FENCE();
  ISSUE(3, 0);
  GQ_DEC(gqa);
  if (!GQ_OK(gqa)) {
    STAMP(11);
#pragma unroll 1
    for (unsigned spin = 0; spin < SPIN_CAP; ++spin) {
      v4u gqb[2];
      GQ_LOAD(gqb);
      GQ_DEC(gqb);
      if (GQ_OK(gqb)) break;
      __builtin_amdgcn_s_sleep(4);
    }
  }
  FENCE();
  {
#pragma unroll
    for (int i = 0; i < 4; ++i) qd[i] = grp8_sum(qd[i]);
    if (lane < 8) {
#pragma unroll
      for (int i = 0; i < 2; ++i) {
        const int col = 32 * w + 16 * i + 2 * lane;
        if (col < 192) qsh[col >> 1] = (unsigned)f2bf(qd[2 * i]) | ((unsigned)f2bf(qd[2 * i + 1]) << 16);
        else if (col < 240) *(v2f*)(hid + col - 192) = v2f{qd[2 * i], qd[2 * i + 1]};
      }
    }
  }
  wg_barrier();
  STAMP(3);
  const int l40 = lane < 40 ? lane : 39;
  v4u w2_r[2];
  w2_r[0] = __builtin_nontemporal_load((const v4u*)(p.w2 + (size_t)(48 * h + hr0) * DM + 320 * chf + 8 * l40));
  w2_r[1] = __builtin_nontemporal_load((const v4u*)(p.w2 + (size_t)(48 * h + hr1) * DM + 320 * chf + 8 * l40));
  FENCE();
  v4u qv[3];
#pragma unroll
  for (int j = 0; j < 3; ++j) qv[j] = *(const v4u*)(qsh + j * 32 + (lane & 7) * 4);
  const float hraw0 = hid[hr0], hraw1 = hid[hr1];

  v2f acc[12];
#pragma unroll
  for (int i = 0; i < 12; ++i) acc[i] = v2f{0.f, 0.f};
  float m = -1e30f, l = 0.f;
#define CONSUME(slot) do { \
    float d0 = 0.f, d1 = 0.f, d2 = 0.f; \
    _Pragma("unroll") for (int e = 0; e < 4; ++e) { d0 = dot2(kb[slot][0][e], qv[0][e], d0); d1 = dot2(kb[slot][1][e], qv[1][e], d1); d2 = dot2(kb[slot][2][e], qv[2][e], d2); } \
    float dsum = (d0 + d1) + d2; \
    dsum += dpp<0xB1>(dsum); dsum += dpp<0x4E>(dsum); dsum += dpp<0x141>(dsum); \
    const float tt_ = rbf(rbf(dsum) * SCALE) * LOG2E; \
    const float mn = fmaxf(m, tt_); \
    const float al = __builtin_amdgcn_exp2f(m - mn), pp = __builtin_amdgcn_exp2f(tt_ - mn); \
    l = l * al + pp; m = mn; \
    const v2f al2 = v2f{al, al}, pp2 = v2f{pp, pp}; \
    _Pragma("unroll") for (int jj = 0; jj < 3; ++jj) _Pragma("unroll") for (int e = 0; e < 4; ++e) { \
      const unsigned u_ = vb[slot][jj][e]; \
      acc[jj * 4 + e] = __builtin_elementwise_fma(pp2, v2f{lo_f(u_), hi_f(u_)}, acc[jj * 4 + e] * al2); } \
    asm volatile("" : "+v"(acc[0]), "+v"(acc[1]), "+v"(acc[2]), "+v"(acc[3]), "+v"(acc[4]), "+v"(acc[5]), "+v"(acc[6]), "+v"(acc[7]), \
                      "+v"(acc[8]), "+v"(acc[9]), "+v"(acc[10]), "+v"(acc[11]), "+v"(m), "+v"(l) :: "memory"); \
    __builtin_amdgcn_sched_barrier(0); } while (0)
  for (int it = 1; it < UPW / RING; ++it) {
#pragma unroll
    for (int d = 0; d < RING; ++d) { CONSUME(d); ISSUE(d, it); }
  }
  {
    float hv[2];
#pragma unroll
    for (int i = 0; i < 2; ++i) {
      const int hr = i ? hr1 : hr0;
      const unsigned bw = i ? b1b : b1a;
      const float hh = rbf(rbf(i ? hraw1 : hraw0) + ((hr & 1) ? hi_f(bw) : lo_f(bw)));
      const float u2 = 2.f * 0.7978845608028654f * (hh + 0.044715f * hh * hh * hh);
      hv[i] = rbf(hh / (1.f + __expf(-u2)));
    }
    if (w >= 4) hv[1] = 0.f;
    float y[8];
#pragma unroll
    for (int e = 0; e < 4; ++e) {
      y[2 * e] = hv[0] * lo_f(w2_r[0][e]) + hv[1] * lo_f(w2_r[1][e]);
      y[2 * e + 1] = hv[0] * hi_f(w2_r[0][e]) + hv[1] * hi_f(w2_r[1][e]);
    }
    if (lane < 40) {
      float4* dst = (float4*)(ysh + w * 320 + 8 * lane);
      dst[0] = make_float4(y[0], y[1], y[2], y[3]); dst[1] = make_float4(y[4], y[5], y[6], y[7]);
    }
  }
  FENCE();
  v4u wo_r[6];
#pragma unroll
  for (int i = 0; i < 6; ++i) wo_r[i] = __builtin_nontemporal_load((const v4u*)(p.wo + (size_t)(DH * h + 48 * rq + 6 * w + i) * DM + 320 * chf + 8 * l40));
  FENCE();
  int pass = 1;
  {
    unsigned c = 1;
    if (lane == 0) c = __hip_atomic_fetch_add(cnt, 1u, __ATOMIC_RELAXED, __HIP_MEMORY_SCOPE_WORKGROUP);
    pass = __builtin_amdgcn_readfirstlane(c) == 0 ? 0 : 1;
  }
  FENCE();
#define HINT() do { unsigned c_ = 0; \
    if (lane == 0) c_ = __hip_atomic_fetch_add(cnt + 1, 1u, __ATOMIC_RELAXED, __HIP_MEMORY_SCOPE_WORKGROUP); \
    c_ = __builtin_amdgcn_readfirstlane(c_); \
    st2(rws, (c_ == NW - 1 && lane == 0) ? WS_CTL + (unsigned)h * CTL_STR + 128u + (unsigned)s * 16u : OOR, 0.f, 0.f, tg); \
    FENCE(); } while (0)
  if (HINT_AT == 0) HINT();
  CONSUME(0);
  if (HINT_AT == 1) HINT();
  CONSUME(1);
  if (HINT_AT == 2) HINT();
  CONSUME(2);
  if (HINT_AT == 3) HINT();
  CONSUME(3);
  STAMP(4);

  {
    const float M = grp8_max(m);
    const float sc = __builtin_amdgcn_exp2f(m - M);
    l = grp8_sum(l * sc);
    float a[24];
#pragma unroll
    for (int i = 0; i < 12; ++i) { a[2 * i] = acc[i].x * sc; a[2 * i + 1] = acc[i].y * sc; }
    float b[12], c[6];
#pragma unroll
    for (int i = 0; i < 12; ++i) b[i] = sw32(a[i], a[i + 12]);
#pragma unroll
    for (int i = 0; i < 6; ++i) c[i] = sw16(b[i], b[i + 6]);
#pragma unroll
    for (int i = 0; i < 6; ++i) c[i] += dpp<0x128>(c[i]);
    if ((lane & 8) == 0) {
      v2f* dst = (v2f*)(mrg + w * 200 + ((lane >> 4) * 8 + (lane & 7)) * 6);
      dst[0] = v2f{c[0], c[1]}; dst[1] = v2f{c[2], c[3]}; dst[2] = v2f{c[4], c[5]};
    }
    if (lane == 0) *(v2f*)(mrg + w * 200 + 192) = v2f{M, l};
  }
#pragma unroll 1
  for (;;) {
    asm volatile("" : "+v"(pass));
    const bool real = __builtin_amdgcn_readfirstlane(pass) != 0;
    const __amdgpu_buffer_rsrc_t rs = __builtin_amdgcn_make_buffer_rsrc((void*)p.ws, 0, real ? (int)WS_END : 0, 0x00020000);
    const int te = real ? t : lane;
    if (real) wg_barrier();
    STAMPR(5);
    if (te < 97) {
      float mw[NW];
      float MM = -1e30f;
#pragma unroll
      for (int i = 0; i < NW; ++i) { mw[i] = mrg[i * 200 + 192]; MM = fmaxf(MM, mw[i]); }
      MM = q24(MM);
      v2f r = v2f{0.f, 0.f};
#pragma unroll
      for (int i = 0; i < NW; ++i) { const float e = __builtin_amdgcn_exp2f(mw[i] - MM); r += v2f{e, e} * *(const v2f*)(mrg + i * 200 + 2 * te); }
      if (te == 96) r.x = MM;
      const int gc = te / 3, k = 2 * (te % 3), a = 6 * (gc >> 3) + k, dim = 64 * (a >> 3) + 8 * (gc & 7) + (a & 7);
      const unsigned off = te < 96 ? WS_PART + (unsigned)bid * 1536u + (unsigned)dim * 8u : WS_CTL + (unsigned)h * CTL_STR + (unsigned)s * 16u;
      st2(rs, off, r.x, r.y, tg);
    }
    {
      const int ty = real ? t - 128 : lane;
      if (ty >= 0 && ty < 160) {
        v2f r = v2f{0.f, 0.f};
#pragma unroll
        for (int i = 0; i < NW; ++i) r += *(const v2f*)(ysh + i * 320 + 2 * ty);
        st2(rs, WS_SP + (unsigned)(256 + bid) * 2560u + (unsigned)ty * 16u, r.x, r.y, tg);
      }
    }
    STAMPR(6);
    if (w == NW - 1 || !real) {
      const unsigned cbase = WS_CTL + (unsigned)h * CTL_STR;
#pragma unroll 1
      for (unsigned spin = 0; spin < SPIN_CAP; ++spin) {
        const v4u g = ld2(rs, cbase + 128u + (unsigned)(lane & 7) * 16u);
        if (__all(mism(g, tg) == 0u) || !real) break;
        __builtin_amdgcn_s_sleep(HINT_SLEEP);
      }
      STAMPR(7);
      unsigned po = WS_PART + (unsigned)(h * 8 + (lane >> 3)) * 1536u + (unsigned)rq * 384u + (unsigned)(lane & 7) * 16u;
      unsigned mo = cbase + (unsigned)(lane >> 3) * 16u;
      float av[6] = {0.f, 0.f, 0.f, 0.f, 0.f, 0.f}, ms = 0.f, ls = 1.f;
#pragma unroll 1
      for (unsigned spin = 0; spin < SPIN_CAP; ++spin) {
        const v4u g0 = ld2(rs, po), g1 = ld2(rs, po + 128u), g2 = ld2(rs, po + 256u), g3 = ld2(rs, mo);
        const bool got = ((mism(g0, tg) | mism(g1, tg)) | (mism(g2, tg) | mism(g3, tg))) == 0u;
        if (got) {
          av[0] = dec_a(g0); av[1] = dec_b(g0); av[2] = dec_a(g1); av[3] = dec_b(g1); av[4] = dec_a(g2); av[5] = dec_b(g2);
          ms = dec_a(g3); ls = dec_b(g3); po = OOR; mo = OOR;
        }
        if (__all(po == OOR) || !real) break;
        __builtin_amdgcn_s_sleep(POLL_SLEEP);
      }
      const float MM = grp8_max(ms);
      const float e = __builtin_amdgcn_exp2f(ms - MM);
      const float wgt = e / grp8_sum(e * ls);
#pragma unroll
      for (int i = 0; i < 6; ++i) av[i] = rbf(grp8_sum(wgt * av[i]));
      if (lane < 8) {
#pragma unroll
        for (int i = 0; i < 3; ++i) *(v2f*)(osh + 16 * i + 2 * lane) = v2f{av[2 * i], av[2 * i + 1]};
      }
    }
    if (real) wg_barrier();
    STAMPR(8);
    {
      asm volatile("" : "+v"(wo_r[0]), "+v"(wo_r[1]), "+v"(wo_r[2]), "+v"(wo_r[3]), "+v"(wo_r[4]), "+v"(wo_r[5]) :: "memory");
      __builtin_amdgcn_sched_barrier(0);
      const v2f o01 = *(const v2f*)(osh + 6 * w), o23 = *(const v2f*)(osh + 6 * w + 2), o45 = *(const v2f*)(osh + 6 * w + 4);
      float a[8];
#pragma unroll
      for (int e = 0; e < 4; ++e) {
        a[2 * e] = ((o01.x * lo_f(wo_r[0][e]) + o01.y * lo_f(wo_r[1][e])) + (o23.x * lo_f(wo_r[2][e]) + o23.y * lo_f(wo_r[3][e]))) + (o45.x * lo_f(wo_r[4][e]) + o45.y * lo_f(wo_r[5][e]));
        a[2 * e + 1] = ((o01.x * hi_f(wo_r[0][e]) + o01.y * hi_f(wo_r[1][e])) + (o23.x * hi_f(wo_r[2][e]) + o23.y * hi_f(wo_r[3][e]))) + (o45.x * hi_f(wo_r[4][e]) + o45.y * hi_f(wo_r[5][e]));
      }
      if (lane < 40) {
        float4* dst = (float4*)(ssh + w * 320 + 8 * lane);
        dst[0] = make_float4(a[0], a[1], a[2], a[3]); dst[1] = make_float4(a[4], a[5], a[6], a[7]);
      }
    }
    if (real) wg_barrier();
    if (te < 160) {
      v2f r = v2f{0.f, 0.f};
#pragma unroll
      for (int i = 0; i < NW; ++i) r += *(const v2f*)(ssh + i * 320 + 2 * te);
      st2(rs, WS_SP + (unsigned)bid * 2560u + (unsigned)te * 16u, r.x, r.y, tg);
    }
    STAMPR(9);
    if (bid < 40 || !real) {
      const int fch = fb >= 20 ? 1 : 0;
      const unsigned coff = (unsigned)(16 * fb - 320 * fch) * 8u + (unsigned)(lane & 7) * 16u;
      unsigned so[4];
#pragma unroll
      for (int i = 0; i < 4; ++i) so[i] = WS_SP + (unsigned)((i >> 1) * 256 + 2 * (16 * w + 8 * (i & 1) + (lane >> 3)) + fch) * 2560u + coff;
      float va[4] = {0.f, 0.f, 0.f, 0.f}, vb2[4] = {0.f, 0.f, 0.f, 0.f};
#pragma unroll 1
      for (unsigned spin = 0; spin < SPIN_CAP; ++spin) {
        v4u g[4];
#pragma unroll
        for (int i = 0; i < 4; ++i) g[i] = ld2(rs, so[i]);
#pragma unroll
        for (int i = 0; i < 4; ++i) if (mism(g[i], tg) == 0u) { va[i] = dec_a(g[i]); vb2[i] = dec_b(g[i]); so[i] = OOR; }
        if (__all(((so[0] & so[1]) & (so[2] & so[3])) == OOR) || !real) break;
        __builtin_amdgcn_s_sleep(POLL_SLEEP);
      }
      const float s0 = grp8_sum(va[0] + va[1]), s1 = grp8_sum(vb2[0] + vb2[1]);
      const float y0 = grp8_sum(va[2] + va[3]), y1 = grp8_sum(vb2[2] + vb2[3]);
      if (lane < 8) *(float4*)(fin + w * 32 + 4 * lane) = make_float4(s0, s1, y0, y1);
      if (real) wg_barrier();
      if (te < 8) {
        float4 r = make_float4(0.f, 0.f, 0.f, 0.f);
#pragma unroll
        for (int i = 0; i < NW; ++i) { const float4 v = *(const float4*)(fin + i * 32 + 4 * te); r.x += v.x; r.y += v.y; r.z += v.z; r.w += v.w; }
        const float ya = rbf(rbf(r.z) + lo_f(b2fin)), yb = rbf(rbf(r.w) + hi_f(b2fin));
        const float ga = rbf(ya / (1.f + __expf(-ya))), gb = rbf(yb / (1.f + __expf(-yb)));
        const unsigned res = (unsigned)f2bf(lo_f(xfin) + rbf(rbf(r.x) * ga)) | ((unsigned)f2bf(hi_f(xfin) + rbf(rbf(r.y) * gb)) << 16);
        if (real) {
          ((unsigned*)p.out)[8 * fb + te] = res;
          if (bid == 0 && te == 0) __hip_atomic_store((u64*)(p.ws + WS_EPOCH), Enew, __ATOMIC_RELAXED, __HIP_MEMORY_SCOPE_AGENT);
        }
      }
    }
    STAMPR(10);
    if (real) break;
    pass = 1;
  }
}

extern "C" void kernel_launch(void* const* d_in, const int* in_sizes, int n_in, void* d_out, int out_size, void* d_ws, size_t ws_size, hipStream_t stream) {
  Params p;
  p.x = (const unsigned short*)d_in[0];
  p.K = (const unsigned char*)d_in[1];
  p.V = (const unsigned char*)d_in[2];
  p.wq = (const unsigned short*)d_in[3];
  p.wo = (const unsigned short*)d_in[4];
  p.w1 = (const unsigned short*)d_in[6];
  p.b1 = (const unsigned short*)d_in[7];
  p.w2 = (const unsigned short*)d_in[8];
  p.b2 = (const unsigned short*)d_in[9];
  p.ws = (unsigned char*)d_ws;
  p.out = (unsigned short*)d_out;
  hipLaunchKernelGGL(fused_kernel, dim3(256), dim3(NT), 0, stream, p);
}
```

```cpp
#include <hip/hip_runtime.h>
#include <hip/hip_bf16.h>
#include <stdint.h>

typedef unsigned v4u __attribute__((ext_vector_type(4)));
typedef unsigned v2u __attribute__((ext_vector_type(2)));
typedef float v2f __attribute__((ext_vector_type(2)));
typedef __bf16 bf16x2 __attribute__((ext_vector_type(2)));
typedef unsigned long long u64;

#define HINT_SLEEP 16
#define POLL_SLEEP 1
#define HINT_AT 1
#define SORDER 1
#define SKEW 0

constexpr int DM = 640, NH = 32, SK = 32768, DH = 192, FF = 1536;
constexpr int NSPLIT = 8, NW = 8, NT = 512, RING = 4;
constexpr int UPW = SK / NSPLIT / 8 / NW;
constexpr size_t HEAD_BYTES = (size_t)SK * DH * 2;
constexpr float SCALE = 0.07216878364870322f;
constexpr float LOG2E = 1.4426950408889634f;
constexpr unsigned SPIN_CAP = 400000u;
constexpr unsigned OOR = 0x7ffff000u;

constexpr unsigned WS_EPOCH = 0;
constexpr unsigned WS_CTL = 4096;
constexpr unsigned CTL_STR = 4352;
constexpr unsigned WS_QX = WS_CTL + 32 * CTL_STR;
constexpr unsigned WS_PART = WS_QX + 256 * 1920;
constexpr unsigned WS_SP = WS_PART + 256 * 1536;
constexpr unsigned WS_END = WS_SP + 2 * 256 * 2560;

__device__ __forceinline__ float rbf(float x) { return (float)(__bf16)x; }
__device__ __forceinline__ unsigned short f2bf(float x) { return __builtin_bit_cast(unsigned short, (__bf16)x); }
__device__ __forceinline__ float lo_f(unsigned u) { return __builtin_bit_cast(float, u << 16); }
__device__ __forceinline__ float hi_f(unsigned u) { return __builtin_bit_cast(float, u & 0xffff0000u); }
__device__ __forceinline__ float dot2(unsigned a, unsigned b, float c) {
  return __builtin_amdgcn_fdot2_f32_bf16(__builtin_bit_cast(bf16x2, a), __builtin_bit_cast(bf16x2, b), c, false);
}
template <int CTRL> __device__ __forceinline__ float dpp(float x) {
  return __builtin_bit_cast(float, __builtin_amdgcn_mov_dpp(__builtin_bit_cast(int, x), CTRL, 0xf, 0xf, true));
}
__device__ __forceinline__ float sw16(float a, float b) {
  auto s = __builtin_amdgcn_permlane16_swap(__float_as_uint(a), __float_as_uint(b), false, false);
  return __uint_as_float(s[0]) + __uint_as_float(s[1]);
}
__device__ __forceinline__ float sw32(float a, float b) {
  auto s = __builtin_amdgcn_permlane32_swap(__float_as_uint(a), __float_as_uint(b), false, false);
  return __uint_as_float(s[0]) + __uint_as_float(s[1]);
}
__device__ __forceinline__ float x16_sum(float x) { return sw16(x, x); }
__device__ __forceinline__ float x32_sum(float x) { return sw32(x, x); }
__device__ __forceinline__ float x16_max(float x) {
  auto s = __builtin_amdgcn_permlane16_swap(__float_as_uint(x), __float_as_uint(x), false, false);
  return fmaxf(__uint_as_float(s[0]), __uint_as_float(s[1]));
}
__device__ __forceinline__ float x32_max(float x) {
  auto s = __builtin_amdgcn_permlane32_swap(__float_as_uint(x), __float_as_uint(x), false, false);
  return fmaxf(__uint_as_float(s[0]), __uint_as_float(s[1]));
}
__device__ __forceinline__ float sum16(float x) {
  x += dpp<0xB1>(x); x += dpp<0x4E>(x); x += dpp<0x141>(x); x += dpp<0x140>(x);
  return x;
}
__device__ __forceinline__ float wave_sum(float x) { return x32_sum(x16_sum(sum16(x))); }
__device__ __forceinline__ float grp8_sum(float x) {
  x += dpp<0x128>(x);
  return x32_sum(x16_sum(x));
}
__device__ __forceinline__ float grp8_max(float x) {
  x = fmaxf(x, dpp<0x128>(x));
  return x32_max(x16_max(x));
}
__device__ __forceinline__ void wg_barrier() {
  asm volatile("s_waitcnt lgkmcnt(0)" ::: "memory");
  __builtin_amdgcn_s_barrier();
  asm volatile("" ::: "memory");
}

struct Tag { unsigned t0, t1; };
__device__ __forceinline__ unsigned f24(float v) { return (__builtin_bit_cast(unsigned, v) + 0x80u) >> 8; }
__device__ __forceinline__ float q24(float v) { return __builtin_bit_cast(float, f24(v) << 8); }
__device__ __forceinline__ v4u enc2(float a, float b, Tag tg) { return v4u{tg.t0 | f24(a), tg.t1, tg.t0 | f24(b), tg.t1}; }
__device__ __forceinline__ unsigned mism(v4u g, Tag tg) {
  return ((g.y ^ tg.t1) | (g.w ^ tg.t1)) | ((((g.x ^ tg.t0) | (g.z ^ tg.t0))) & 0xff000000u);
}
__device__ __forceinline__ float dec_a(v4u g) { return __builtin_bit_cast(float, g.x << 8); }
__device__ __forceinline__ float dec_b(v4u g) { return __builtin_bit_cast(float, g.z << 8); }
__device__ __forceinline__ void st2(__amdgpu_buffer_rsrc_t rs, unsigned off, float a, float b, Tag tg) {
  __builtin_amdgcn_raw_buffer_store_b128(enc2(a, b, tg), rs, off, 0, 16);
}
__device__ __forceinline__ v4u ld2(__amdgpu_buffer_rsrc_t rs, unsigned off) {
  return __builtin_amdgcn_raw_buffer_load_b128(rs, off, 0, 16);
}

struct Params {
  const unsigned short* x; const unsigned char* K; const unsigned char* V; const unsigned short* wq; const unsigned short* wo;
  const unsigned short* w1; const unsigned short* b1; const unsigned short* w2; const unsigned short* b2;
  unsigned char* ws; unsigned short* out;
};

#define STAMP(i) do {} while (0)
#define STAMPR(i) do {} while (0)
#define FENCE() do { asm volatile("" ::: "memory"); __builtin_amdgcn_sched_barrier(0); } while (0)

__global__ void __launch_bounds__(NT, 2) fused_kernel(Params p) {
  __shared__ __attribute__((aligned(16))) float lds[16 * 240 + 8 * 320 + 8 * 200 + 8 * 32 + 48 + 48 + 96 + 16];
  float* red = lds;
  float* ysh = lds;
  float* ssh = lds + 16 * 240;
  float* mrg = ssh + 8 * 320;
  float* fin = mrg + 8 * 200;
  float* osh = fin + 8 * 32;
  float* hid = osh + 48;
  unsigned* qsh = (unsigned*)(hid + 48);
  unsigned* cnt = qsh + 96;

  const int bid = blockIdx.x, h = bid >> 3, s = bid & 7;
  const int rq = s >> 1, chf = s & 1;
  const int t = threadIdx.x, lane = t & 63, w = __builtin_amdgcn_readfirstlane(t >> 6);
  STAMP(0);
  const __amdgpu_buffer_rsrc_t rws = __builtin_amdgcn_make_buffer_rsrc((void*)p.ws, 0, (int)WS_END, 0x00020000);
  const __amdgpu_buffer_rsrc_t rk = __builtin_amdgcn_make_buffer_rsrc((void*)(p.K + (size_t)h * HEAD_BYTES), 0, (int)HEAD_BYTES, 0x00020000);
  const __amdgpu_buffer_rsrc_t rv = __builtin_amdgcn_make_buffer_rsrc((void*)(p.V + (size_t)h * HEAD_BYTES), 0, (int)HEAD_BYTES, 0x00020000);

  const unsigned loff = (lane >> 3) * 384 + (lane & 7) * 16;
  const unsigned ubase = (unsigned)(s * NW + w) * (RING * 3072u);
  v4u kb[RING][3], vb[RING][3];
#define ISSUE(slot, base) do { const unsigned so_ = (base) + (slot) * 3072u; \
    _Pragma("unroll") for (int jj = 0; jj < 3; ++jj) kb[slot][jj] = __builtin_amdgcn_raw_buffer_load_b128(rk, loff + jj * 128, so_, 2); \
    _Pragma("unroll") for (int jj = 0; jj < 3; ++jj) vb[slot][jj] = __builtin_amdgcn_raw_buffer_load_b128(rv, loff + jj * 128, so_, 2); \
    FENCE(); } while (0)
  ISSUE(0, ubase);
  unsigned xw[5];
#pragma unroll
  for (int i = 0; i < 5; ++i) xw[i] = ((const unsigned*)p.x)[lane + 64 * i];
  const int tt = t < 480 ? t : 479;
  const int rg = tt / 30, ch = tt % 30;
  unsigned xr[5];
  v4u wv[5];
  {
    const bool isq = ch < 24;
    const unsigned short* wsel = isq ? p.wq : p.w1;
    const unsigned ldw = isq ? NH * DH : FF;
    const unsigned coff = isq ? DH * h + 8 * ch : 48 * h + 8 * (ch - 24);
    const unsigned short* base = wsel + (size_t)((80 * s + rg) * ldw + coff);
#pragma unroll
    for (int i = 0; i < 5; ++i) wv[i] = __builtin_nontemporal_load((const v4u*)(base + (size_t)(i * 16 * ldw)));
#pragma unroll
    for (int i = 0; i < 5; ++i) xr[i] = ((const unsigned*)p.x)[(80 * s + rg + 16 * i) >> 1];
  }
  unsigned zero_v;
  asm volatile("v_mov_b32 %0, 0" : "=v"(zero_v));
  v2u Ev = __builtin_amdgcn_raw_buffer_load_b64(rws, WS_EPOCH + zero_v, 0, 16);
  const int hr0 = 12 * rq + w, hr1 = 12 * rq + 8 + (w & 3);
  if (t == 0) cnt[0] = 0u;
  FENCE();
  STAMP(12);
  __builtin_amdgcn_s_barrier();
  FENCE();
  STAMP(13);
  ISSUE(1, ubase);
  asm volatile("" : "+v"(Ev), "+v"(xw[0]), "+v"(xw[1]), "+v"(xw[2]), "+v"(xw[3]), "+v"(xw[4]) :: "memory");
  __builtin_amdgcn_sched_barrier(0);
  STAMP(14);
  Tag tg;
  u64 Enew;
  {
    const u64 E = ((u64)__builtin_amdgcn_readfirstlane(Ev.y) << 32) | (unsigned)__builtin_amdgcn_readfirstlane(Ev.x);
    u64 z = E + 0x9E3779B97F4A7C15ull;
    z = (z ^ (z >> 30)) * 0xBF58476D1CE4E5B9ull; z = (z ^ (z >> 27)) * 0x94D049BB133111EBull; z ^= z >> 31;
    Enew = z; tg.t0 = (unsigned)z << 24; tg.t1 = (unsigned)(z >> 8) | 1u;
  }
  {
    float sum = 0.f;
#pragma unroll
    for (int i = 0; i < 5; ++i) sum += lo_f(xw[i]) + hi_f(xw[i]);
    const float mean = wave_sum(sum) * (1.f / DM);
    float var = 0.f;
#pragma unroll
    for (int i = 0; i < 5; ++i) { const float a = lo_f(xw[i]) - mean, b = hi_f(xw[i]) - mean; var += a * a + b * b; }
    var = wave_sum(var) * (1.f / DM);
    const float mb = rbf(mean), den = rbf(sqrtf(rbf(rbf(var) + 1e-5f)));
    float a8[8] = {0.f, 0.f, 0.f, 0.f, 0.f, 0.f, 0.f, 0.f};
#pragma unroll
    for (int i = 0; i < 5; ++i) {
      const float xe = (rg & 1) ? hi_f(xr[i]) : lo_f(xr[i]);
      const float xn = rbf(rbf(xe - mb) / den);
#pragma unroll
      for (int e = 0; e < 4; ++e) { a8[2 * e] += xn * lo_f(wv[i][e]); a8[2 * e + 1] += xn * hi_f(wv[i][e]); }
    }
    if (t < 480) {
      float4* dst = (float4*)(red + rg * 240 + ch * 8);
      dst[0] = make_float4(a8[0], a8[1], a8[2], a8[3]); dst[1] = make_float4(a8[4], a8[5], a8[6], a8[7]);
    }
  }
  STAMP(1);
  wg_barrier();
  if (t < 120) {
    v2f r = v2f{0.f, 0.f};
#pragma unroll
    for (int i = 0; i < 16; ++i) r += *(const v2f*)(red + i * 240 + 2 * t);
    st2(rws, WS_QX + (unsigned)bid * 1920u + (unsigned)t * 16u, r.x, r.y, tg);
  }
  FENCE();
  __builtin_amdgcn_s_barrier();
  FENCE();
  STAMP(2);
  const unsigned gq_off = WS_QX + (unsigned)(h * 8 + (lane >> 3)) * 1920u + (unsigned)w * 256u + (unsigned)(lane & 7) * 16u;
  const unsigned gq1 = w < 7 ? 128u : 0u;
#define GQ_LOAD(G) do { G[0] = ld2(rws, gq_off); G[1] = ld2(rws, gq_off + gq1); FENCE(); } while (0)
#define GQ_OK(G) __all((mism(G[0], tg) | mism(G[1], tg)) == 0u)
#define GQ_DEC(G) do { qd[0] = dec_a(G[0]); qd[1] = dec_b(G[0]); qd[2] = dec_a(G[1]); qd[3] = dec_b(G[1]); } while (0)
  v4u gqa[2];
  float qd[4];
  ISSUE(2, ubase);
  const int l40 = lane < 40 ? lane : 39;
  v4u w2_r[2];
  w2_r[0] = __builtin_nontemporal_load((const v4u*)(p.w2 + (size_t)(48 * h + hr0) * DM + 320 * chf + 8 * l40));
  w2_r[1] = __builtin_nontemporal_load((const v4u*)(p.w2 + (size_t)(48 * h + hr1) * DM + 320 * chf + 8 * l40));
  const unsigned b1a = ((const unsigned*)p.b1)[((48 * h + hr0) >> 1) + zero_v], b1b = ((const unsigned*)p.b1)[((48 * h + hr1) >> 1) + zero_v];
  FENCE();
  __builtin_amdgcn_s_barrier();
  FENCE();
  GQ_LOAD(gqa);
  __builtin_amdgcn_s_barrier();
  FENCE();
  ISSUE(3, ubase);
  GQ_DEC(gqa);
  if (!GQ_OK(gqa)) {
    STAMP(11);
#pragma unroll 1
    for (unsigned spin = 0; spin < SPIN_CAP; ++spin) {
      v4u gqb[2];
      GQ_LOAD(gqb);
      GQ_DEC(gqb);
      if (GQ_OK(gqb)) break;
      __builtin_amdgcn_s_sleep(4);
    }
  }
  FENCE();
  {
#pragma unroll
    for (int i = 0; i < 4; ++i) qd[i] = grp8_sum(qd[i]);
    if (lane < 8) {
#pragma unroll
      for (int i = 0; i < 2; ++i) {
        const int col = 32 * w + 16 * i + 2 * lane;
        if (col < 192) qsh[col >> 1] = (unsigned)f2bf(qd[2 * i]) | ((unsigned)f2bf(qd[2 * i + 1]) << 16);
        else if (col < 240) *(v2f*)(hid + col - 192) = v2f{qd[2 * i], qd[2 * i + 1]};
      }
    }
  }
  wg_barrier();
  STAMP(3);
  v4u qv[3];
#pragma unroll
  for (int j = 0; j < 3; ++j) qv[j] = *(const v4u*)(qsh + j * 32 + (lane & 7) * 4);
  const float hraw0 = hid[hr0], hraw1 = hid[hr1];

  {
    float hv[2];
#pragma unroll
    for (int i = 0; i < 2; ++i) {
      const int hr = i ? hr1 : hr0;
      const unsigned bw = i ? b1b : b1a;
      const float hh = rbf(rbf(i ? hraw1 : hraw0) + ((hr & 1) ? hi_f(bw) : lo_f(bw)));
      const float u2 = 2.f * 0.7978845608028654f * (hh + 0.044715f * hh * hh * hh);
      hv[i] = rbf(hh / (1.f + __expf(-u2)));
    }
    if (w >= 4) hv[1] = 0.f;
    float y[8];
#pragma unroll
    for (int e = 0; e < 4; ++e) {
      y[2 * e] = hv[0] * lo_f(w2_r[0][e]) + hv[1] * lo_f(w2_r[1][e]);
      y[2 * e + 1] = hv[0] * hi_f(w2_r[0][e]) + hv[1] * hi_f(w2_r[1][e]);
    }
    if (lane < 40) {
      float4* dst = (float4*)(ysh + w * 320 + 8 * lane);
      dst[0] = make_float4(y[0], y[1], y[2], y[3]); dst[1] = make_float4(y[4], y[5], y[6], y[7]);
    }
  }
  FENCE();

  v2f acc[12];
#pragma unroll
  for (int i = 0; i < 12; ++i) acc[i] = v2f{0.f, 0.f};
  float m = -1e30f, l = 0.f;
#define CONSUME(slot) do { \
    float d0 = 0.f, d1 = 0.f, d2 = 0.f; \
    _Pragma("unroll") for (int e = 0; e < 4; ++e) { d0 = dot2(kb[slot][0][e], qv[0][e], d0); d1 = dot2(kb[slot][1][e], qv[1][e], d1); d2 = dot2(kb[slot][2][e], qv[2][e], d2); } \
    float dsum = (d0 + d1) + d2; \
    dsum += dpp<0xB1>(dsum); dsum += dpp<0x4E>(dsum); dsum += dpp<0x141>(dsum); \
    const float tt_ = rbf(rbf(dsum) * SCALE) * LOG2E; \
    const float mn = fmaxf(m, tt_); \
    const float al = __builtin_amdgcn_exp2f(m - mn), pp = __builtin_amdgcn_exp2f(tt_ - mn); \
    l = l * al + pp; m = mn; \
    const v2f al2 = v2f{al, al}, pp2 = v2f{pp, pp}; \
    _Pragma("unroll") for (int jj = 0; jj < 3; ++jj) _Pragma("unroll") for (int e = 0; e < 4; ++e) { \
      const unsigned u_ = vb[slot][jj][e]; \
      acc[jj * 4 + e] = __builtin_elementwise_fma(pp2, v2f{lo_f(u_), hi_f(u_)}, acc[jj * 4 + e] * al2); } \
    asm volatile("" : "+v"(acc[0]), "+v"(acc[1]), "+v"(acc[2]), "+v"(acc[3]), "+v"(acc[4]), "+v"(acc[5]), "+v"(acc[6]), "+v"(acc[7]), \
                      "+v"(acc[8]), "+v"(acc[9]), "+v"(acc[10]), "+v"(acc[11]), "+v"(m), "+v"(l) :: "memory"); \
    __builtin_amdgcn_sched_barrier(0); } while (0)
  constexpr int NIT = UPW / RING;
  constexpr unsigned ITSTR = NSPLIT * NW * RING * 3072u;
  const int nit = NIT + (w < SKEW ? ((s & 1) ? -1 : 1) : 0);
  for (int it = 1; it < nit; ++it) {
    const unsigned base = it < NIT ? ubase + (unsigned)it * ITSTR : ubase + (NIT - 1) * ITSTR + NW * RING * 3072u;
#pragma unroll
    for (int d = 0; d < RING; ++d) { CONSUME(d); ISSUE(d, base); }
  }
  const int fb = bid < 40 ? bid : 0;
  const unsigned xfin = ((const unsigned*)p.x)[8 * fb + (lane & 7)], b2fin = ((const unsigned*)p.b2)[8 * fb + (lane & 7)];
  v4u wo_r[6];
#pragma unroll
  for (int i = 0; i < 6; ++i) wo_r[i] = __builtin_nontemporal_load((const v4u*)(p.wo + (size_t)(DH * h + 48 * rq + 6 * w + i) * DM + 320 * chf + 8 * l40));
  FENCE();
#define HINT() do { unsigned c_ = 0; \
    if (lane == 0) c_ = __hip_atomic_fetch_add(cnt, 1u, __ATOMIC_RELAXED, __HIP_MEMORY_SCOPE_WORKGROUP); \
    c_ = __builtin_amdgcn_readfirstlane(c_); \
    st2(rws, (c_ == NW - 1 && lane == 0) ? WS_CTL + (unsigned)h * CTL_STR + 128u + (unsigned)s * 16u : OOR, 0.f, 0.f, tg); \
    FENCE(); } while (0)
  if (HINT_AT == 0) HINT();
  CONSUME(0);
  if (HINT_AT == 1) HINT();
  CONSUME(1);
  if (HINT_AT == 2) HINT();
  CONSUME(2);
  if (HINT_AT == 3) HINT();
  CONSUME(3);
  STAMP(4);

  {
    const float M = grp8_max(m);
    const float sc = __builtin_amdgcn_exp2f(m - M);
    l = grp8_sum(l * sc);
    float a[24];
#pragma unroll
    for (int i = 0; i < 12; ++i) { a[2 * i] = acc[i].x * sc; a[2 * i + 1] = acc[i].y * sc; }
    float b[12], c[6];
#pragma unroll
    for (int i = 0; i < 12; ++i) b[i] = sw32(a[i], a[i + 12]);
#pragma unroll
    for (int i = 0; i < 6; ++i) c[i] = sw16(b[i], b[i + 6]);
#pragma unroll
    for (int i = 0; i < 6; ++i) c[i] += dpp<0x128>(c[i]);
    if ((lane & 8) == 0) {
      v2f* dst = (v2f*)(mrg + w * 200 + ((lane >> 4) * 8 + (lane & 7)) * 6);
      dst[0] = v2f{c[0], c[1]}; dst[1] = v2f{c[2], c[3]}; dst[2] = v2f{c[4], c[5]};
    }
    if (lane == 0) *(v2f*)(mrg + w * 200 + 192) = v2f{M, l};
  }
  wg_barrier();
  STAMP(5);
  if (t < 97) {
    float mw[NW];
    float MM = -1e30f;
#pragma unroll
    for (int i = 0; i < NW; ++i) { mw[i] = mrg[i * 200 + 192]; MM = fmaxf(MM, mw[i]); }
    MM = q24(MM);
    v2f r = v2f{0.f, 0.f};
#pragma unroll
    for (int i = 0; i < NW; ++i) { const float e = __builtin_amdgcn_exp2f(mw[i] - MM); r += v2f{e, e} * *(const v2f*)(mrg + i * 200 + 2 * t); }
    if (t == 96) r.x = MM;
    const int gc = t / 3, k = 2 * (t % 3), a = 6 * (gc >> 3) + k, dim = 64 * (a >> 3) + 8 * (gc & 7) + (a & 7);
    const unsigned off = t < 96 ? WS_PART + (unsigned)bid * 1536u + (unsigned)dim * 8u : WS_CTL + (unsigned)h * CTL_STR + (unsigned)s * 16u;
    st2(rws, off, r.x, r.y, tg);
  }
  if (t >= 128 && t < 288) {
    const int ty = t - 128;
    v2f r = v2f{0.f, 0.f};
#pragma unroll
    for (int i = 0; i < NW; ++i) r += *(const v2f*)(ysh + i * 320 + 2 * ty);
    st2(rws, WS_SP + (unsigned)(256 + bid) * 2560u + (unsigned)ty * 16u, r.x, r.y, tg);
  }
  FENCE();
  STAMP(6);
  v2f wf[6][4];
#pragma unroll
  for (int i = 0; i < 6; ++i)
#pragma unroll
    for (int e = 0; e < 4; ++e) wf[i][e] = v2f{lo_f(wo_r[i][e]), hi_f(wo_r[i][e])};
#pragma unroll
  for (int i = 0; i < 6; ++i) asm volatile("" : "+v"(wf[i][0]), "+v"(wf[i][1]), "+v"(wf[i][2]), "+v"(wf[i][3]));
  FENCE();
  if (w == NW - 1) {
    const unsigned cbase = WS_CTL + (unsigned)h * CTL_STR;
#pragma unroll 1
    for (unsigned spin = 0; spin < SPIN_CAP; ++spin) {
      const v4u g = ld2(rws, cbase + 128u + (unsigned)(lane & 7) * 16u);
      if (__all(mism(g, tg) == 0u)) break;
      __builtin_amdgcn_s_sleep(HINT_SLEEP);
    }
    STAMP(7);
    unsigned po = WS_PART + (unsigned)(h * 8 + (lane >> 3)) * 1536u + (unsigned)rq * 384u + (unsigned)(lane & 7) * 16u;
    unsigned mo = cbase + (unsigned)(lane >> 3) * 16u;
    float av[6] = {0.f, 0.f, 0.f, 0.f, 0.f, 0.f}, ms = 0.f, ls = 1.f;
#pragma unroll 1
    for (unsigned spin = 0; spin < SPIN_CAP; ++spin) {
      const v4u g0 = ld2(rws, po), g1 = ld2(rws, po + 128u), g2 = ld2(rws, po + 256u), g3 = ld2(rws, mo);
      const bool got = ((mism(g0, tg) | mism(g1, tg)) | (mism(g2, tg) | mism(g3, tg))) == 0u;
      if (got) {
        av[0] = dec_a(g0); av[1] = dec_b(g0); av[2] = dec_a(g1); av[3] = dec_b(g1); av[4] = dec_a(g2); av[5] = dec_b(g2);
        ms = dec_a(g3); ls = dec_b(g3); po = OOR; mo = OOR;
      }
      if (__all(po == OOR)) break;
      __builtin_amdgcn_s_sleep(POLL_SLEEP);
    }
    const float MM = grp8_max(ms);
    const float e = __builtin_amdgcn_exp2f(ms - MM);
    const float wgt = e / grp8_sum(e * ls);
#pragma unroll
    for (int i = 0; i < 6; ++i) av[i] = rbf(grp8_sum(wgt * av[i]));
    if (lane < 8) {
#pragma unroll
      for (int i = 0; i < 3; ++i) *(v2f*)(osh + 16 * i + 2 * lane) = v2f{av[2 * i], av[2 * i + 1]};
    }
  }
  FENCE();
  wg_barrier();
  STAMP(8);
  {
    const v2f o01 = *(const v2f*)(osh + 6 * w), o23 = *(const v2f*)(osh + 6 * w + 2), o45 = *(const v2f*)(osh + 6 * w + 4);
    v2f a[4];
#pragma unroll
    for (int e = 0; e < 4; ++e) {
      const v2f u0 = __builtin_elementwise_fma(v2f{o01.y, o01.y}, wf[1][e], v2f{o01.x, o01.x} * wf[0][e]);
      const v2f u1 = __builtin_elementwise_fma(v2f{o23.y, o23.y}, wf[3][e], v2f{o23.x, o23.x} * wf[2][e]);
      const v2f u2 = __builtin_elementwise_fma(v2f{o45.y, o45.y}, wf[5][e], v2f{o45.x, o45.x} * wf[4][e]);
      a[e] = (u0 + u1) + u2;
    }
    if (lane < 40) {
      float4* dst = (float4*)(ssh + w * 320 + 8 * lane);
      dst[0] = make_float4(a[0].x, a[0].y, a[1].x, a[1].y); dst[1] = make_float4(a[2].x, a[2].y, a[3].x, a[3].y);
    }
  }
  wg_barrier();
  if (t < 160) {
    v2f r = v2f{0.f, 0.f};
#pragma unroll
    for (int i = 0; i < NW; ++i) r += *(const v2f*)(ssh + i * 320 + 2 * t);
    st2(rws, WS_SP + (unsigned)bid * 2560u + (unsigned)t * 16u, r.x, r.y, tg);
  }
  STAMP(9);
  if (bid < 40) {
    const int fch = fb >= 20 ? 1 : 0;
    const unsigned coff = (unsigned)(16 * fb - 320 * fch) * 8u + (unsigned)(lane & 7) * 16u;
    unsigned so[4];
#pragma unroll
    for (int i = 0; i < 4; ++i) so[i] = WS_SP + (unsigned)((i >> 1) * 256 + 2 * (16 * w + 8 * (i & 1) + (lane >> 3)) + fch) * 2560u + coff;
    float va[4] = {0.f, 0.f, 0.f, 0.f}, vb2[4] = {0.f, 0.f, 0.f, 0.f};
#pragma unroll 1
    for (unsigned spin = 0; spin < SPIN_CAP; ++spin) {
      v4u g[4];
#pragma unroll
      for (int i = 0; i < 4; ++i) g[i] = ld2(rws, so[i]);
#pragma unroll
      for (int i = 0; i < 4; ++i) if (mism(g[i], tg) == 0u) { va[i] = dec_a(g[i]); vb2[i] = dec_b(g[i]); so[i] = OOR; }
      if (__all(((so[0] & so[1]) & (so[2] & so[3])) == OOR)) break;
      __builtin_amdgcn_s_sleep(POLL_SLEEP);
    }
    const float s0 = grp8_sum(va[0] + va[1]), s1 = grp8_sum(vb2[0] + vb2[1]);
    const float y0 = grp8_sum(va[2] + va[3]), y1 = grp8_sum(vb2[2] + vb2[3]);
    if (lane < 8) *(float4*)(fin + w * 32 + 4 * lane) = make_float4(s0, s1, y0, y1);
    wg_barrier();
    if (t < 8) {
      float4 r = make_float4(0.f, 0.f, 0.f, 0.f);
#pragma unroll
      for (int i = 0; i < NW; ++i) { const float4 v = *(const float4*)(fin + i * 32 + 4 * t); r.x += v.x; r.y += v.y; r.z += v.z; r.w += v.w; }
      const float ya = rbf(rbf(r.z) + lo_f(b2fin)), yb = rbf(rbf(r.w) + hi_f(b2fin));
      const float ga = rbf(ya / (1.f + __expf(-ya))), gb = rbf(yb / (1.f + __expf(-yb)));
      const unsigned res = (unsigned)f2bf(lo_f(xfin) + rbf(rbf(r.x) * ga)) | ((unsigned)f2bf(hi_f(xfin) + rbf(rbf(r.y) * gb)) << 16);
      ((unsigned*)p.out)[8 * fb + t] = res;
      if (bid == 0 && t == 0) __hip_atomic_store((u64*)(p.ws + WS_EPOCH), Enew, __ATOMIC_RELAXED, __HIP_MEMORY_SCOPE_AGENT);
    }
  }
  STAMP(10);
}

extern "C" void kernel_launch(void* const* d_in, const int* in_sizes, int n_in, void* d_out, int out_size, void* d_ws, size_t ws_size, hipStream_t stream) {
  Params p;
  p.x = (const unsigned short*)d_in[0];
  p.K = (const unsigned char*)d_in[1];
  p.V = (const unsigned char*)d_in[2];
  p.wq = (const unsigned short*)d_in[3];
  p.wo = (const unsigned short*)d_in[4];
  p.w1 = (const unsigned short*)d_in[6];
  p.b1 = (const unsigned short*)d_in[7];
  p.w2 = (const unsigned short*)d_in[8];
  p.b2 = (const unsigned short*)d_in[9];
  p.ws = (unsigned char*)d_ws;
  p.out = (unsigned short*)d_out;
  hipLaunchKernelGGL(fused_kernel, dim3(256), dim3(NT), 0, stream, p);
}
```

```cpp
#include <hip/hip_runtime.h>
#include <hip/hip_bf16.h>
#include <stdint.h>

typedef unsigned v4u __attribute__((ext_vector_type(4)));
typedef unsigned v2u __attribute__((ext_vector_type(2)));
typedef float v2f __attribute__((ext_vector_type(2)));
typedef __bf16 bf16x2 __attribute__((ext_vector_type(2)));
typedef unsigned long long u64;

#define HINT_SLEEP 16
#define POLL_SLEEP 1
#define HINT_AT 1
#define HMAP 1
#define FIN_SLEEP 16
#define WOPOS 0
#define KVAUX 2
#define SORDER 1
#define SKEW 0

constexpr int DM = 640, NH = 32, SK = 32768, DH = 192, FF = 1536;
constexpr int NSPLIT = 8, NW = 8, NT = 512, RING = 4;
constexpr int UPW = SK / NSPLIT / 8 / NW;
constexpr size_t HEAD_BYTES = (size_t)SK * DH * 2;
constexpr float SCALE = 0.07216878364870322f;
constexpr float LOG2E = 1.4426950408889634f;
constexpr unsigned SPIN_CAP = 400000u;
constexpr unsigned OOR = 0x7ffff000u;

constexpr unsigned WS_EPOCH = 0;
constexpr unsigned WS_CTL = 4096;
constexpr unsigned CTL_STR = 4352;
constexpr unsigned WS_QX = WS_CTL + 32 * CTL_STR;
constexpr unsigned WS_PART = WS_QX + 256 * 1920;
constexpr unsigned WS_SP = WS_PART + 256 * 1536;
constexpr unsigned WS_END = WS_SP + 2 * 256 * 2560;

__device__ __forceinline__ float rbf(float x) { return (float)(__bf16)x; }
__device__ __forceinline__ unsigned short f2bf(float x) { return __builtin_bit_cast(unsigned short, (__bf16)x); }
__device__ __forceinline__ float lo_f(unsigned u) { return __builtin_bit_cast(float, u << 16); }
__device__ __forceinline__ float hi_f(unsigned u) { return __builtin_bit_cast(float, u & 0xffff0000u); }
__device__ __forceinline__ float dot2(unsigned a, unsigned b, float c) {
  return __builtin_amdgcn_fdot2_f32_bf16(__builtin_bit_cast(bf16x2, a), __builtin_bit_cast(bf16x2, b), c, false);
}
template <int CTRL> __device__ __forceinline__ float dpp(float x) {
  return __builtin_bit_cast(float, __builtin_amdgcn_mov_dpp(__builtin_bit_cast(int, x), CTRL, 0xf, 0xf, true));
}
__device__ __forceinline__ float sw16(float a, float b) {
  auto s = __builtin_amdgcn_permlane16_swap(__float_as_uint(a), __float_as_uint(b), false, false);
  return __uint_as_float(s[0]) + __uint_as_float(s[1]);
}
__device__ __forceinline__ float sw32(float a, float b) {
  auto s = __builtin_amdgcn_permlane32_swap(__float_as_uint(a), __float_as_uint(b), false, false);
  return __uint_as_float(s[0]) + __uint_as_float(s[1]);
}
__device__ __forceinline__ float x16_sum(float x) { return sw16(x, x); }
__device__ __forceinline__ float x32_sum(float x) { return sw32(x, x); }
__device__ __forceinline__ float x16_max(float x) {
  auto s = __builtin_amdgcn_permlane16_swap(__float_as_uint(x), __float_as_uint(x), false, false);
  return fmaxf(__uint_as_float(s[0]), __uint_as_float(s[1]));
}
__device__ __forceinline__ float x32_max(float x) {
  auto s = __builtin_amdgcn_permlane32_swap(__float_as_uint(x), __float_as_uint(x), false, false);
  return fmaxf(__uint_as_float(s[0]), __uint_as_float(s[1]));
}
__device__ __forceinline__ float sum16(float x) {
  x += dpp<0xB1>(x); x += dpp<0x4E>(x); x += dpp<0x141>(x); x += dpp<0x140>(x);
  return x;
}
__device__ __forceinline__ float wave_sum(float x) { return x32_sum(x16_sum(sum16(x))); }
__device__ __forceinline__ float grp8_sum(float x) {
  x += dpp<0x128>(x);
  return x32_sum(x16_sum(x));
}
__device__ __forceinline__ float grp8_max(float x) {
  x = fmaxf(x, dpp<0x128>(x));
  return x32_max(x16_max(x));
}
__device__ __forceinline__ void wg_barrier() {
  asm volatile("s_waitcnt lgkmcnt(0)" ::: "memory");
  __builtin_amdgcn_s_barrier();
  asm volatile("" ::: "memory");
}

struct Tag { unsigned t0, t1; };
__device__ __forceinline__ unsigned f24(float v) { return (__builtin_bit_cast(unsigned, v) + 0x80u) >> 8; }
__device__ __forceinline__ float q24(float v) { return __builtin_bit_cast(float, f24(v) << 8); }
__device__ __forceinline__ v4u enc2(float a, float b, Tag tg) { return v4u{tg.t0 | f24(a), tg.t1, tg.t0 | f24(b), tg.t1}; }
__device__ __forceinline__ unsigned mism(v4u g, Tag tg) {
  return ((g.y ^ tg.t1) | (g.w ^ tg.t1)) | ((((g.x ^ tg.t0) | (g.z ^ tg.t0))) & 0xff000000u);
}
__device__ __forceinline__ float dec_a(v4u g) { return __builtin_bit_cast(float, g.x << 8); }
__device__ __forceinline__ float dec_b(v4u g) { return __builtin_bit_cast(float, g.z << 8); }
__device__ __forceinline__ void st2(__amdgpu_buffer_rsrc_t rs, unsigned off, float a, float b, Tag tg) {
  __builtin_amdgcn_raw_buffer_store_b128(enc2(a, b, tg), rs, off, 0, 16);
}
__device__ __forceinline__ v4u ld2(__amdgpu_buffer_rsrc_t rs, unsigned off) {
  return __builtin_amdgcn_raw_buffer_load_b128(rs, off, 0, 16);
}

struct Params {
  const unsigned short* x; const unsigned char* K; const unsigned char* V; const unsigned short* wq; const unsigned short* wo;
  const unsigned short* w1; const unsigned short* b1; const unsigned short* w2; const unsigned short* b2;
  unsigned char* ws; unsigned short* out;
};

#define STAMP(i) do {} while (0)
#define STAMPR(i) do {} while (0)
#define FENCE() do { asm volatile("" ::: "memory"); __builtin_amdgcn_sched_barrier(0); } while (0)

__global__ void __launch_bounds__(NT, 2) fused_kernel(Params p) {
  __shared__ __attribute__((aligned(16))) float lds[16 * 240 + 8 * 320 + 8 * 200 + 8 * 32 + 48 + 48 + 96 + 16];
  float* red = lds;
  float* ysh = lds;
  float* ssh = lds + 16 * 240;
  float* mrg = ssh + 8 * 320;
  float* fin = mrg + 8 * 200;
  float* osh = fin + 8 * 32;
  float* hid = osh + 48;
  unsigned* qsh = (unsigned*)(hid + 48);
  unsigned* cnt = qsh + 96;

  const int bid = (4 * (blockIdx.x & 7) + ((blockIdx.x >> 3) & 3)) * 8 + (blockIdx.x >> 5);
  const int h = bid >> 3, s = bid & 7;
  const int rq = s >> 1, chf = s & 1;
  const int t = threadIdx.x, lane = t & 63, w = __builtin_amdgcn_readfirstlane(t >> 6);
  STAMP(0);
  const __amdgpu_buffer_rsrc_t rws = __builtin_amdgcn_make_buffer_rsrc((void*)p.ws, 0, (int)WS_END, 0x00020000);
  const __amdgpu_buffer_rsrc_t rk = __builtin_amdgcn_make_buffer_rsrc((void*)(p.K + (size_t)h * HEAD_BYTES), 0, (int)HEAD_BYTES, 0x00020000);
  const __amdgpu_buffer_rsrc_t rv = __builtin_amdgcn_make_buffer_rsrc((void*)(p.V + (size_t)h * HEAD_BYTES), 0, (int)HEAD_BYTES, 0x00020000);

  const unsigned loff = (lane >> 3) * 384 + (lane & 7) * 16;
  const unsigned ubase = (unsigned)(s * NW + w) * (RING * 3072u);
  v4u kb[RING][3], vb[RING][3];
#define ISSUE(slot, base) do { const unsigned so_ = (base) + (slot) * 3072u; \
    _Pragma("unroll") for (int jj = 0; jj < 3; ++jj) kb[slot][jj] = __builtin_amdgcn_raw_buffer_load_b128(rk, loff + jj * 128, so_, KVAUX); \
    _Pragma("unroll") for (int jj = 0; jj < 3; ++jj) vb[slot][jj] = __builtin_amdgcn_raw_buffer_load_b128(rv, loff + jj * 128, so_, KVAUX); \
    FENCE(); } while (0)
  ISSUE(0, ubase);
  unsigned xw[5];
#pragma unroll
  for (int i = 0; i < 5; ++i) xw[i] = ((const unsigned*)p.x)[lane + 64 * i];
  const int tt = t < 480 ? t : 479;
  const int rg = tt / 30, ch = tt % 30;
  unsigned xr[5];
  v4u wv[5];
  {
    const bool isq = ch < 24;
    const unsigned short* wsel = isq ? p.wq : p.w1;
    const unsigned ldw = isq ? NH * DH : FF;
    const unsigned coff = isq ? DH * h + 8 * ch : 48 * h + 8 * (ch - 24);
    const unsigned short* base = wsel + (size_t)((80 * s + rg) * ldw + coff);
#pragma unroll
    for (int i = 0; i < 5; ++i) wv[i] = __builtin_nontemporal_load((const v4u*)(base + (size_t)(i * 16 * ldw)));
#pragma unroll
    for (int i = 0; i < 5; ++i) xr[i] = ((const unsigned*)p.x)[(80 * s + rg + 16 * i) >> 1];
  }
  unsigned zero_v;
  asm volatile("v_mov_b32 %0, 0" : "=v"(zero_v));
  v2u Ev = __builtin_amdgcn_raw_buffer_load_b64(rws, WS_EPOCH + zero_v, 0, 16);
  const int hr0 = 12 * rq + w, hr1 = 12 * rq + 8 + (w & 3);
  if (t == 0) cnt[0] = 0u;
  FENCE();
  STAMP(12);
  __builtin_amdgcn_s_barrier();
  FENCE();
  STAMP(13);
  ISSUE(1, ubase);
  asm volatile("" : "+v"(Ev), "+v"(xw[0]), "+v"(xw[1]), "+v"(xw[2]), "+v"(xw[3]), "+v"(xw[4]) :: "memory");
  __builtin_amdgcn_sched_barrier(0);
  STAMP(14);
  Tag tg;
  u64 Enew;
  {
    const u64 E = ((u64)__builtin_amdgcn_readfirstlane(Ev.y) << 32) | (unsigned)__builtin_amdgcn_readfirstlane(Ev.x);
    u64 z = E + 0x9E3779B97F4A7C15ull;
    z = (z ^ (z >> 30)) * 0xBF58476D1CE4E5B9ull; z = (z ^ (z >> 27)) * 0x94D049BB133111EBull; z ^= z >> 31;
    Enew = z; tg.t0 = (unsigned)z << 24; tg.t1 = (unsigned)(z >> 8) | 1u;
  }
  {
    float sum = 0.f;
#pragma unroll
    for (int i = 0; i < 5; ++i) sum += lo_f(xw[i]) + hi_f(xw[i]);
    const float mean = wave_sum(sum) * (1.f / DM);
    float var = 0.f;
#pragma unroll
    for (int i = 0; i < 5; ++i) { const float a = lo_f(xw[i]) - mean, b = hi_f(xw[i]) - mean; var += a * a + b * b; }
    var = wave_sum(var) * (1.f / DM);
    const float mb = rbf(mean), den = rbf(sqrtf(rbf(rbf(var) + 1e-5f)));
    float a8[8] = {0.f, 0.f, 0.f, 0.f, 0.f, 0.f, 0.f, 0.f};
#pragma unroll
    for (int i = 0; i < 5; ++i) {
      const float xe = (rg & 1) ? hi_f(xr[i]) : lo_f(xr[i]);
      const float xn = rbf(rbf(xe - mb) / den);
#pragma unroll
      for (int e = 0; e < 4; ++e) { a8[2 * e] += xn * lo_f(wv[i][e]); a8[2 * e + 1] += xn * hi_f(wv[i][e]); }
    }
    if (t < 480) {
      float4* dst = (float4*)(red + rg * 240 + ch * 8);
      dst[0] = make_float4(a8[0], a8[1], a8[2], a8[3]); dst[1] = make_float4(a8[4], a8[5], a8[6], a8[7]);
    }
  }
  STAMP(1);
  wg_barrier();
  if (t < 120) {
    v2f r = v2f{0.f, 0.f};
#pragma unroll
    for (int i = 0; i < 16; ++i) r += *(const v2f*)(red + i * 240 + 2 * t);
    st2(rws, WS_QX + (unsigned)bid * 1920u + (unsigned)t * 16u, r.x, r.y, tg);
  }
  FENCE();
  __builtin_amdgcn_s_barrier();
  FENCE();
  STAMP(2);
  const unsigned gq_off = WS_QX + (unsigned)(h * 8 + (lane >> 3)) * 1920u + (unsigned)w * 256u + (unsigned)(lane & 7) * 16u;
  const unsigned gq1 = w < 7 ? 128u : 0u;
#define GQ_LOAD(G) do { G[0] = ld2(rws, gq_off); G[1] = ld2(rws, gq_off + gq1); FENCE(); } while (0)
#define GQ_OK(G) __all((mism(G[0], tg) | mism(G[1], tg)) == 0u)
#define GQ_DEC(G) do { qd[0] = dec_a(G[0]); qd[1] = dec_b(G[0]); qd[2] = dec_a(G[1]); qd[3] = dec_b(G[1]); } while (0)
  v4u gqa[2];
  float qd[4];
  ISSUE(2, ubase);
  const int l40 = lane < 40 ? lane : 39;
  v4u w2_r[2];
  w2_r[0] = __builtin_nontemporal_load((const v4u*)(p.w2 + (size_t)(48 * h + hr0) * DM + 320 * chf + 8 * l40));
  w2_r[1] = __builtin_nontemporal_load((const v4u*)(p.w2 + (size_t)(48 * h + hr1) * DM + 320 * chf + 8 * l40));
  const unsigned b1a = ((const unsigned*)p.b1)[((48 * h + hr0) >> 1) + zero_v], b1b = ((const unsigned*)p.b1)[((48 * h + hr1) >> 1) + zero_v];
  FENCE();
  __builtin_amdgcn_s_barrier();
  FENCE();
  GQ_LOAD(gqa);
  __builtin_amdgcn_s_barrier();
  FENCE();
  ISSUE(3, ubase);
  GQ_DEC(gqa);
  if (!GQ_OK(gqa)) {
    STAMP(11);
#pragma unroll 1
    for (unsigned spin = 0; spin < SPIN_CAP; ++spin) {
      v4u gqb[2];
      GQ_LOAD(gqb);
      GQ_DEC(gqb);
      if (GQ_OK(gqb)) break;
      __builtin_amdgcn_s_sleep(4);
    }
  }
  FENCE();
  {
#pragma unroll
    for (int i = 0; i < 4; ++i) qd[i] = grp8_sum(qd[i]);
    if (lane < 8) {
#pragma unroll
      for (int i = 0; i < 2; ++i) {
        const int col = 32 * w + 16 * i + 2 * lane;
        if (col < 192) qsh[col >> 1] = (unsigned)f2bf(qd[2 * i]) | ((unsigned)f2bf(qd[2 * i + 1]) << 16);
        else if (col < 240) *(v2f*)(hid + col - 192) = v2f{qd[2 * i], qd[2 * i + 1]};
      }
    }
  }
  wg_barrier();
  STAMP(3);
  v4u qv[3];
#pragma unroll
  for (int j = 0; j < 3; ++j) qv[j] = *(const v4u*)(qsh + j * 32 + (lane & 7) * 4);
  const float hraw0 = hid[hr0], hraw1 = hid[hr1];

  {
    float hv[2];
#pragma unroll
    for (int i = 0; i < 2; ++i) {
      const int hr = i ? hr1 : hr0;
      const unsigned bw = i ? b1b : b1a;
      const float hh = rbf(rbf(i ? hraw1 : hraw0) + ((hr & 1) ? hi_f(bw) : lo_f(bw)));
      const float u2 = 2.f * 0.7978845608028654f * (hh + 0.044715f * hh * hh * hh);
      hv[i] = rbf(hh / (1.f + __expf(-u2)));
    }
    if (w >= 4) hv[1] = 0.f;
    float y[8];
#pragma unroll
    for (int e = 0; e < 4; ++e) {
      y[2 * e] = hv[0] * lo_f(w2_r[0][e]) + hv[1] * lo_f(w2_r[1][e]);
      y[2 * e + 1] = hv[0] * hi_f(w2_r[0][e]) + hv[1] * hi_f(w2_r[1][e]);
    }
    if (lane < 40) {
      float4* dst = (float4*)(ysh + w * 320 + 8 * lane);
      dst[0] = make_float4(y[0], y[1], y[2], y[3]); dst[1] = make_float4(y[4], y[5], y[6], y[7]);
    }
  }
  FENCE();

  v2f acc[12];
#pragma unroll
  for (int i = 0; i < 12; ++i) acc[i] = v2f{0.f, 0.f};
  float m = -1e30f, l = 0.f;
#define CONSUME(slot) do { \
    float d0 = 0.f, d1 = 0.f, d2 = 0.f; \
    _Pragma("unroll") for (int e = 0; e < 4; ++e) { d0 = dot2(kb[slot][0][e], qv[0][e], d0); d1 = dot2(kb[slot][1][e], qv[1][e], d1); d2 = dot2(kb[slot][2][e], qv[2][e], d2); } \
    float dsum = (d0 + d1) + d2; \
    dsum += dpp<0xB1>(dsum); dsum += dpp<0x4E>(dsum); dsum += dpp<0x141>(dsum); \
    const float tt_ = rbf(rbf(dsum) * SCALE) * LOG2E; \
    const float mn = fmaxf(m, tt_); \
    const float al = __builtin_amdgcn_exp2f(m - mn), pp = __builtin_amdgcn_exp2f(tt_ - mn); \
    l = l * al + pp; m = mn; \
    const v2f al2 = v2f{al, al}, pp2 = v2f{pp, pp}; \
    _Pragma("unroll") for (int jj = 0; jj < 3; ++jj) _Pragma("unroll") for (int e = 0; e < 4; ++e) { \
      const unsigned u_ = vb[slot][jj][e]; \
      acc[jj * 4 + e] = __builtin_elementwise_fma(pp2, v2f{lo_f(u_), hi_f(u_)}, acc[jj * 4 + e] * al2); } \
    asm volatile("" : "+v"(acc[0]), "+v"(acc[1]), "+v"(acc[2]), "+v"(acc[3]), "+v"(acc[4]), "+v"(acc[5]), "+v"(acc[6]), "+v"(acc[7]), \
                      "+v"(acc[8]), "+v"(acc[9]), "+v"(acc[10]), "+v"(acc[11]), "+v"(m), "+v"(l) :: "memory"); \
    __builtin_amdgcn_sched_barrier(0); } while (0)
  constexpr int NIT = UPW / RING;
  constexpr unsigned ITSTR = NSPLIT * NW * RING * 3072u;
  const int nit = NIT + (w < SKEW ? ((s & 1) ? -1 : 1) : 0);
  for (int it = 1; it < nit; ++it) {
    const unsigned base = it < NIT ? ubase + (unsigned)it * ITSTR : ubase + (NIT - 1) * ITSTR + NW * RING * 3072u;
#pragma unroll
    for (int d = 0; d < RING; ++d) { CONSUME(d); ISSUE(d, base); }
  }
  const int fb = bid < 40 ? bid : 0;
  unsigned xfin, b2fin;
  v4u wo_r[6];
#define LOAD_WO() do { \
    xfin = ((const unsigned*)p.x)[8 * fb + (lane & 7)]; b2fin = ((const unsigned*)p.b2)[8 * fb + (lane & 7)]; \
    _Pragma("unroll") for (int i = 0; i < 6; ++i) wo_r[i] = __builtin_nontemporal_load((const v4u*)(p.wo + (size_t)(DH * h + 48 * rq + 6 * w + i) * DM + 320 * chf + 8 * l40)); \
    FENCE(); } while (0)
  if (WOPOS == 0) LOAD_WO();
#define HINT() do { unsigned c_ = 0; \
    if (lane == 0) c_ = __hip_atomic_fetch_add(cnt, 1u, __ATOMIC_RELAXED, __HIP_MEMORY_SCOPE_WORKGROUP); \
    c_ = __builtin_amdgcn_readfirstlane(c_); \
    st2(rws, (c_ == NW - 1 && lane == 0) ? WS_CTL + (unsigned)h * CTL_STR + 128u + (unsigned)s * 16u : OOR, 0.f, 0.f, tg); \
    FENCE(); } while (0)
  if (HINT_AT == 0) HINT();
  CONSUME(0);
  if (HINT_AT == 1) HINT();
  CONSUME(1);
  if (HINT_AT == 2) HINT();
  if (WOPOS == 1) LOAD_WO();
  CONSUME(2);
  if (HINT_AT == 3) HINT();
  CONSUME(3);
  if (WOPOS == 2) LOAD_WO();
  STAMP(4);

  {
    const float M = grp8_max(m);
    const float sc = __builtin_amdgcn_exp2f(m - M);
    l = grp8_sum(l * sc);
    float a[24];
#pragma unroll
    for (int i = 0; i < 12; ++i) { a[2 * i] = acc[i].x * sc; a[2 * i + 1] = acc[i].y * sc; }
    float b[12], c[6];
#pragma unroll
    for (int i = 0; i < 12; ++i) b[i] = sw32(a[i], a[i + 12]);
#pragma unroll
    for (int i = 0; i < 6; ++i) c[i] = sw16(b[i], b[i + 6]);
#pragma unroll
    for (int i = 0; i < 6; ++i) c[i] += dpp<0x128>(c[i]);
    if ((lane & 8) == 0) {
      v2f* dst = (v2f*)(mrg + w * 200 + ((lane >> 4) * 8 + (lane & 7)) * 6);
      dst[0] = v2f{c[0], c[1]}; dst[1] = v2f{c[2], c[3]}; dst[2] = v2f{c[4], c[5]};
    }
    if (lane == 0) *(v2f*)(mrg + w * 200 + 192) = v2f{M, l};
  }
  wg_barrier();
  STAMP(5);
  if (t < 97) {
    float mw[NW];
    float MM = -1e30f;
#pragma unroll
    for (int i = 0; i < NW; ++i) { mw[i] = mrg[i * 200 + 192]; MM = fmaxf(MM, mw[i]); }
    MM = q24(MM);
    v2f r = v2f{0.f, 0.f};
#pragma unroll
    for (int i = 0; i < NW; ++i) { const float e = __builtin_amdgcn_exp2f(mw[i] - MM); r += v2f{e, e} * *(const v2f*)(mrg + i * 200 + 2 * t); }
    if (t == 96) r.x = MM;
    const int gc = t / 3, k = 2 * (t % 3), a = 6 * (gc >> 3) + k, dim = 64 * (a >> 3) + 8 * (gc & 7) + (a & 7);
    const unsigned off = t < 96 ? WS_PART + (unsigned)bid * 1536u + (unsigned)dim * 8u : WS_CTL + (unsigned)h * CTL_STR + (unsigned)s * 16u;
    st2(rws, off, r.x, r.y, tg);
  }
  if (t >= 128 && t < 288) {
    const int ty = t - 128;
    v2f r = v2f{0.f, 0.f};
#pragma unroll
    for (int i = 0; i < NW; ++i) r += *(const v2f*)(ysh + i * 320 + 2 * ty);
    st2(rws, WS_SP + (unsigned)(256 + bid) * 2560u + (unsigned)ty * 16u, r.x, r.y, tg);
  }
  FENCE();
  STAMP(6);
  v2f wf[6][4];
#pragma unroll
  for (int i = 0; i < 6; ++i)
#pragma unroll
    for (int e = 0; e < 4; ++e) wf[i][e] = v2f{lo_f(wo_r[i][e]), hi_f(wo_r[i][e])};
#pragma unroll
  for (int i = 0; i < 6; ++i) asm volatile("" : "+v"(wf[i][0]), "+v"(wf[i][1]), "+v"(wf[i][2]), "+v"(wf[i][3]));
  FENCE();
  if (w == NW - 1) {
    const unsigned cbase = WS_CTL + (unsigned)h * CTL_STR;
#pragma unroll 1
    for (unsigned spin = 0; spin < SPIN_CAP; ++spin) {
      const v4u g = ld2(rws, cbase + 128u + (unsigned)(lane & 7) * 16u);
      if (__all(mism(g, tg) == 0u)) break;
      __builtin_amdgcn_s_sleep(HINT_SLEEP);
    }
    STAMP(7);
    unsigned po = WS_PART + (unsigned)(h * 8 + (lane >> 3)) * 1536u + (unsigned)rq * 384u + (unsigned)(lane & 7) * 16u;
    unsigned mo = cbase + (unsigned)(lane >> 3) * 16u;
    float av[6] = {0.f, 0.f, 0.f, 0.f, 0.f, 0.f}, ms = 0.f, ls = 1.f;
#pragma unroll 1
    for (unsigned spin = 0; spin < SPIN_CAP; ++spin) {
      const v4u g0 = ld2(rws, po), g1 = ld2(rws, po + 128u), g2 = ld2(rws, po + 256u), g3 = ld2(rws, mo);
      const bool got = ((mism(g0, tg) | mism(g1, tg)) | (mism(g2, tg) | mism(g3, tg))) == 0u;
      if (got) {
        av[0] = dec_a(g0); av[1] = dec_b(g0); av[2] = dec_a(g1); av[3] = dec_b(g1); av[4] = dec_a(g2); av[5] = dec_b(g2);
        ms = dec_a(g3); ls = dec_b(g3); po = OOR; mo = OOR;
      }
      if (__all(po == OOR)) break;
      __builtin_amdgcn_s_sleep(POLL_SLEEP);
    }
    const float MM = grp8_max(ms);
    const float e = __builtin_amdgcn_exp2f(ms - MM);
    const float wgt = e / grp8_sum(e * ls);
#pragma unroll
    for (int i = 0; i < 6; ++i) av[i] = rbf(grp8_sum(wgt * av[i]));
    if (lane < 8) {
#pragma unroll
      for (int i = 0; i < 3; ++i) *(v2f*)(osh + 16 * i + 2 * lane) = v2f{av[2 * i], av[2 * i + 1]};
    }
  }
  FENCE();
  wg_barrier();
  STAMP(8);
  {
    const v2f o01 = *(const v2f*)(osh + 6 * w), o23 = *(const v2f*)(osh + 6 * w + 2), o45 = *(const v2f*)(osh + 6 * w + 4);
    v2f a[4];
#pragma unroll
    for (int e = 0; e < 4; ++e) {
      const v2f u0 = __builtin_elementwise_fma(v2f{o01.y, o01.y}, wf[1][e], v2f{o01.x, o01.x} * wf[0][e]);
      const v2f u1 = __builtin_elementwise_fma(v2f{o23.y, o23.y}, wf[3][e], v2f{o23.x, o23.x} * wf[2][e]);
      const v2f u2 = __builtin_elementwise_fma(v2f{o45.y, o45.y}, wf[5][e], v2f{o45.x, o45.x} * wf[4][e]);
      a[e] = (u0 + u1) + u2;
    }
    if (lane < 40) {
      float4* dst = (float4*)(ssh + w * 320 + 8 * lane);
      dst[0] = make_float4(a[0].x, a[0].y, a[1].x, a[1].y); dst[1] = make_float4(a[2].x, a[2].y, a[3].x, a[3].y);
    }
  }
  wg_barrier();
  if (t < 160) {
    v2f r = v2f{0.f, 0.f};
#pragma unroll
    for (int i = 0; i < NW; ++i) r += *(const v2f*)(ssh + i * 320 + 2 * t);
    st2(rws, WS_SP + (unsigned)bid * 2560u + (unsigned)t * 16u, r.x, r.y, tg);
  }
  STAMP(9);
  if (bid < 40) {
    const int fch = fb >= 20 ? 1 : 0;
    const unsigned coff = (unsigned)(16 * fb - 320 * fch) * 8u + (unsigned)(lane & 7) * 16u;
    unsigned so[4];
#pragma unroll
    for (int i = 0; i < 4; ++i) so[i] = WS_SP + (unsigned)((i >> 1) * 256 + 2 * (16 * w + 8 * (i & 1) + (lane >> 3)) + fch) * 2560u + coff;
    float va[4] = {0.f, 0.f, 0.f, 0.f}, vb2[4] = {0.f, 0.f, 0.f, 0.f};
#pragma unroll 1
    for (unsigned spin = 0; spin < SPIN_CAP; ++spin) {
      v4u g[4];
#pragma unroll
      for (int i = 0; i < 4; ++i) g[i] = ld2(rws, so[i]);
#pragma unroll
      for (int i = 0; i < 4; ++i) if (mism(g[i], tg) == 0u) { va[i] = dec_a(g[i]); vb2[i] = dec_b(g[i]); so[i] = OOR; }
      if (__all(((so[0] & so[1]) & (so[2] & so[3])) == OOR)) break;
      if (__all((so[2] & so[3]) == OOR)) __builtin_amdgcn_s_sleep(POLL_SLEEP); else __builtin_amdgcn_s_sleep(FIN_SLEEP);
    }
    const float s0 = grp8_sum(va[0] + va[1]), s1 = grp8_sum(vb2[0] + vb2[1]);
    const float y0 = grp8_sum(va[2] + va[3]), y1 = grp8_sum(vb2[2] + vb2[3]);
    if (lane < 8) *(float4*)(fin + w * 32 + 4 * lane) = make_float4(s0, s1, y0, y1);
    wg_barrier();
    if (t < 8) {
      float4 r = make_float4(0.f, 0.f, 0.f, 0.f);
#pragma unroll
      for (int i = 0; i < NW; ++i) { const float4 v = *(const float4*)(fin + i * 32 + 4 * t); r.x += v.x; r.y += v.y; r.z += v.z; r.w += v.w; }
      const float ya = rbf(rbf(r.z) + lo_f(b2fin)), yb = rbf(rbf(r.w) + hi_f(b2fin));
      const float ga = rbf(ya / (1.f + __expf(-ya))), gb = rbf(yb / (1.f + __expf(-yb)));
      const unsigned res = (unsigned)f2bf(lo_f(xfin) + rbf(rbf(r.x) * ga)) | ((unsigned)f2bf(hi_f(xfin) + rbf(rbf(r.y) * gb)) << 16);
      ((unsigned*)p.out)[8 * fb + t] = res;
      if (bid == 0 && t == 0) __hip_atomic_store((u64*)(p.ws + WS_EPOCH), Enew, __ATOMIC_RELAXED, __HIP_MEMORY_SCOPE_AGENT);
    }
  }
  STAMP(10);
}

extern "C" void kernel_launch(void* const* d_in, const int* in_sizes, int n_in, void* d_out, int out_size, void* d_ws, size_t ws_size, hipStream_t stream) {
  Params p;
  p.x = (const unsigned short*)d_in[0];
  p.K = (const unsigned char*)d_in[1];
  p.V = (const unsigned char*)d_in[2];
  p.wq = (const unsigned short*)d_in[3];
  p.wo = (const unsigned short*)d_in[4];
  p.w1 = (const unsigned short*)d_in[6];
  p.b1 = (const unsigned short*)d_in[7];
  p.w2 = (const unsigned short*)d_in[8];
  p.b2 = (const unsigned short*)d_in[9];
  p.ws = (unsigned char*)d_ws;
  p.out = (unsigned short*)d_out;
  hipLaunchKernelGGL(fused_kernel, dim3(256), dim3(NT), 0, stream, p);
}
```

```cpp
#include <hip/hip_runtime.h>
#include <hip/hip_bf16.h>
#include <stdint.h>

typedef unsigned v4u __attribute__((ext_vector_type(4)));
typedef unsigned v2u __attribute__((ext_vector_type(2)));
typedef float v2f __attribute__((ext_vector_type(2)));
typedef __bf16 bf16x2 __attribute__((ext_vector_type(2)));
typedef unsigned long long u64;

#define HINT_SLEEP 16
#define POLL_SLEEP 1
#define HINT_AT 2
#define FIN_SLEEP 12
#define HMAP 2
#define NSTAT 14

constexpr int DM = 640, NH = 32, SK = 32768, DH = 192, FF = 1536;
constexpr int NSPLIT = 8, NW = 8, NT = 512, RING = 4;
constexpr int UPW = SK / NSPLIT / 8 / NW;
constexpr size_t HEAD_BYTES = (size_t)SK * DH * 2;
constexpr float SCALE = 0.07216878364870322f;
constexpr float LOG2E = 1.4426950408889634f;
constexpr unsigned SPIN_CAP = 400000u;
constexpr unsigned OOR = 0x7ffff000u;

constexpr unsigned WS_EPOCH = 0;
constexpr int NSC = SK / 256;
constexpr int NREC = NSPLIT + NSC - NSPLIT * NSTAT;
constexpr int NRND = NREC / 8;
static_assert(NREC % 8 == 0 && NREC * 16 <= 1024, "records");
constexpr unsigned WS_CTL = 4096;
constexpr unsigned CTL_STR = 4352;
constexpr unsigned WS_QX = WS_CTL + 32 * CTL_STR;
constexpr unsigned WS_PART = WS_QX + 256 * 1920;
constexpr unsigned WS_SP = WS_PART + 32 * NREC * 1536;
constexpr unsigned WS_END = WS_SP + 2 * 256 * 2560;

__device__ __forceinline__ float rbf(float x) { return (float)(__bf16)x; }
__device__ __forceinline__ unsigned short f2bf(float x) { return __builtin_bit_cast(unsigned short, (__bf16)x); }
__device__ __forceinline__ float lo_f(unsigned u) { return __builtin_bit_cast(float, u << 16); }
__device__ __forceinline__ float hi_f(unsigned u) { return __builtin_bit_cast(float, u & 0xffff0000u); }
__device__ __forceinline__ float dot2(unsigned a, unsigned b, float c) {
  return __builtin_amdgcn_fdot2_f32_bf16(__builtin_bit_cast(bf16x2, a), __builtin_bit_cast(bf16x2, b), c, false);
}
template <int CTRL> __device__ __forceinline__ float dpp(float x) {
  return __builtin_bit_cast(float, __builtin_amdgcn_mov_dpp(__builtin_bit_cast(int, x), CTRL, 0xf, 0xf, true));
}
__device__ __forceinline__ float sw16(float a, float b) {
  auto s = __builtin_amdgcn_permlane16_swap(__float_as_uint(a), __float_as_uint(b), false, false);
  return __uint_as_float(s[0]) + __uint_as_float(s[1]);
}
__device__ __forceinline__ float sw32(float a, float b) {
  auto s = __builtin_amdgcn_permlane32_swap(__float_as_uint(a), __float_as_uint(b), false, false);
  return __uint_as_float(s[0]) + __uint_as_float(s[1]);
}
__device__ __forceinline__ float x16_sum(float x) { return sw16(x, x); }
__device__ __forceinline__ float x32_sum(float x) { return sw32(x, x); }
__device__ __forceinline__ float x16_max(float x) {
  auto s = __builtin_amdgcn_permlane16_swap(__float_as_uint(x), __float_as_uint(x), false, false);
  return fmaxf(__uint_as_float(s[0]), __uint_as_float(s[1]));
}
__device__ __forceinline__ float x32_max(float x) {
  auto s = __builtin_amdgcn_permlane32_swap(__float_as_uint(x), __float_as_uint(x), false, false);
  return fmaxf(__uint_as_float(s[0]), __uint_as_float(s[1]));
}
__device__ __forceinline__ float sum16(float x) {
  x += dpp<0xB1>(x); x += dpp<0x4E>(x); x += dpp<0x141>(x); x += dpp<0x140>(x);
  return x;
}
__device__ __forceinline__ float wave_sum(float x) { return x32_sum(x16_sum(sum16(x))); }
__device__ __forceinline__ float grp8_sum(float x) {
  x += dpp<0x128>(x);
  return x32_sum(x16_sum(x));
}
__device__ __forceinline__ float grp8_max(float x) {
  x = fmaxf(x, dpp<0x128>(x));
  return x32_max(x16_max(x));
}
__device__ __forceinline__ void wg_barrier() {
  asm volatile("s_waitcnt lgkmcnt(0)" ::: "memory");
  __builtin_amdgcn_s_barrier();
  asm volatile("" ::: "memory");
}

struct Tag { unsigned t0, t1; };
__device__ __forceinline__ unsigned f24(float v) { return (__builtin_bit_cast(unsigned, v) + 0x80u) >> 8; }
__device__ __forceinline__ float q24(float v) { return __builtin_bit_cast(float, f24(v) << 8); }
__device__ __forceinline__ v4u enc2(float a, float b, Tag tg) { return v4u{tg.t0 | f24(a), tg.t1, tg.t0 | f24(b), tg.t1}; }
__device__ __forceinline__ unsigned mism(v4u g, Tag tg) {
  return ((g.y ^ tg.t1) | (g.w ^ tg.t1)) | ((((g.x ^ tg.t0) | (g.z ^ tg.t0))) & 0xff000000u);
}
__device__ __forceinline__ float dec_a(v4u g) { return __builtin_bit_cast(float, g.x << 8); }
__device__ __forceinline__ float dec_b(v4u g) { return __builtin_bit_cast(float, g.z << 8); }
__device__ __forceinline__ void st2(__amdgpu_buffer_rsrc_t rs, unsigned off, float a, float b, Tag tg) {
  __builtin_amdgcn_raw_buffer_store_b128(enc2(a, b, tg), rs, off, 0, 16);
}
__device__ __forceinline__ v4u ld2(__amdgpu_buffer_rsrc_t rs, unsigned off) {
  return __builtin_amdgcn_raw_buffer_load_b128(rs, off, 0, 16);
}

struct Params {
  const unsigned short* x; const unsigned char* K; const unsigned char* V; const unsigned short* wq; const unsigned short* wo;
  const unsigned short* w1; const unsigned short* b1; const unsigned short* w2; const unsigned short* b2;
  unsigned char* ws; unsigned short* out;
};

#define STAMP(i) do {} while (0)
#define STAMPR(i) do {} while (0)
#define FENCE() do { asm volatile("" ::: "memory"); __builtin_amdgcn_sched_barrier(0); } while (0)

__global__ void __launch_bounds__(NT, 2) fused_kernel(Params p) {
  __shared__ __attribute__((aligned(16))) float lds[16 * 240 + 8 * 320 + 2 * 8 * 200 + 8 * 32 + 48 + 48 + 96 + 16];
  float* red = lds;
  float* ysh = lds;
  float* ssh = lds + 16 * 240;
  float* mrg = ssh + 8 * 320;
  float* fin = mrg + 2 * 8 * 200;
  float* osh = fin + 8 * 32;
  float* hid = osh + 48;
  unsigned* qsh = (unsigned*)(hid + 48);
  unsigned* cnt = qsh + 96;

  const int bid = (((blockIdx.x & 7) >> 1) + 4 * (blockIdx.x >> 5)) * 8 + ((blockIdx.x >> 3) & 3) * 2 + (blockIdx.x & 1);
  const int h = bid >> 3, s = bid & 7;
  const int rq = s >> 1, chf = s & 1;
  const int t = threadIdx.x, lane = t & 63, w = __builtin_amdgcn_readfirstlane(t >> 6);
  STAMP(0);
  const __amdgpu_buffer_rsrc_t rws = __builtin_amdgcn_make_buffer_rsrc((void*)p.ws, 0, (int)WS_END, 0x00020000);
  const __amdgpu_buffer_rsrc_t rk = __builtin_amdgcn_make_buffer_rsrc((void*)(p.K + (size_t)h * HEAD_BYTES), 0, (int)HEAD_BYTES, 0x00020000);
  const __amdgpu_buffer_rsrc_t rv = __builtin_amdgcn_make_buffer_rsrc((void*)(p.V + (size_t)h * HEAD_BYTES), 0, (int)HEAD_BYTES, 0x00020000);

  const unsigned loff = (lane >> 3) * 384 + (lane & 7) * 16;
  const unsigned ubase = (unsigned)(s * NW + w) * (RING * 3072u);
  v4u kb[RING][3], vb[RING][3];
#define ISSUE(slot, base) do { const unsigned so_ = (base) + (slot) * 3072u; \
    _Pragma("unroll") for (int jj = 0; jj < 3; ++jj) kb[slot][jj] = __builtin_amdgcn_raw_buffer_load_b128(rk, loff + jj * 128, so_, 2); \
    _Pragma("unroll") for (int jj = 0; jj < 3; ++jj) vb[slot][jj] = __builtin_amdgcn_raw_buffer_load_b128(rv, loff + jj * 128, so_, 2); \
    FENCE(); } while (0)
  ISSUE(0, ubase);
  __builtin_amdgcn_raw_buffer_store_b32(0u, rws, (s == 0 && t == 0) ? WS_CTL + (unsigned)h * CTL_STR + 1280u : OOR, 0, 16);
  FENCE();
  unsigned xw[5];
#pragma unroll
  for (int i = 0; i < 5; ++i) xw[i] = ((const unsigned*)p.x)[lane + 64 * i];
  const int tt = t < 480 ? t : 479;
  const int rg = tt / 30, ch = tt % 30;
  unsigned xr[5];
  v4u wv[5];
  {
    const bool isq = ch < 24;
    const unsigned short* wsel = isq ? p.wq : p.w1;
    const unsigned ldw = isq ? NH * DH : FF;
    const unsigned coff = isq ? DH * h + 8 * ch : 48 * h + 8 * (ch - 24);
    const unsigned short* base = wsel + (size_t)((80 * s + rg) * ldw + coff);
#pragma unroll
    for (int i = 0; i < 5; ++i) wv[i] = __builtin_nontemporal_load((const v4u*)(base + (size_t)(i * 16 * ldw)));
#pragma unroll
    for (int i = 0; i < 5; ++i) xr[i] = ((const unsigned*)p.x)[(80 * s + rg + 16 * i) >> 1];
  }
  unsigned zero_v;
  asm volatile("v_mov_b32 %0, 0" : "=v"(zero_v));
  v2u Ev = __builtin_amdgcn_raw_buffer_load_b64(rws, WS_EPOCH + zero_v, 0, 16);
  const int hr0 = 12 * rq + w, hr1 = 12 * rq + 8 + (w & 3);
  if (t == 0) cnt[0] = 0u;
  FENCE();
  STAMP(12);
  __builtin_amdgcn_s_barrier();
  FENCE();
  STAMP(13);
  ISSUE(1, ubase);
  asm volatile("" : "+v"(Ev), "+v"(xw[0]), "+v"(xw[1]), "+v"(xw[2]), "+v"(xw[3]), "+v"(xw[4]) :: "memory");
  __builtin_amdgcn_sched_barrier(0);
  STAMP(14);
  Tag tg;
  u64 Enew;
  {
    const u64 E = ((u64)__builtin_amdgcn_readfirstlane(Ev.y) << 32) | (unsigned)__builtin_amdgcn_readfirstlane(Ev.x);
    u64 z = E + 0x9E3779B97F4A7C15ull;
    z = (z ^ (z >> 30)) * 0xBF58476D1CE4E5B9ull; z = (z ^ (z >> 27)) * 0x94D049BB133111EBull; z ^= z >> 31;
    Enew = z; tg.t0 = (unsigned)z << 24; tg.t1 = (unsigned)(z >> 8) | 1u;
  }
  {
    float sum = 0.f;
#pragma unroll
    for (int i = 0; i < 5; ++i) sum += lo_f(xw[i]) + hi_f(xw[i]);
    const float mean = wave_sum(sum) * (1.f / DM);
    float var = 0.f;
#pragma unroll
    for (int i = 0; i < 5; ++i) { const float a = lo_f(xw[i]) - mean, b = hi_f(xw[i]) - mean; var += a * a + b * b; }
    var = wave_sum(var) * (1.f / DM);
    const float mb = rbf(mean), den = rbf(sqrtf(rbf(rbf(var) + 1e-5f)));
    float a8[8] = {0.f, 0.f, 0.f, 0.f, 0.f, 0.f, 0.f, 0.f};
#pragma unroll
    for (int i = 0; i < 5; ++i) {
      const float xe = (rg & 1) ? hi_f(xr[i]) : lo_f(xr[i]);
      const float xn = rbf(rbf(xe - mb) / den);
#pragma unroll
      for (int e = 0; e < 4; ++e) { a8[2 * e] += xn * lo_f(wv[i][e]); a8[2 * e + 1] += xn * hi_f(wv[i][e]); }
    }
    if (t < 480) {
      float4* dst = (float4*)(red + rg * 240 + ch * 8);
      dst[0] = make_float4(a8[0], a8[1], a8[2], a8[3]); dst[1] = make_float4(a8[4], a8[5], a8[6], a8[7]);
    }
  }
  STAMP(1);
  wg_barrier();
  if (t < 120) {
    v2f r = v2f{0.f, 0.f};
#pragma unroll
    for (int i = 0; i < 16; ++i) r += *(const v2f*)(red + i * 240 + 2 * t);
    st2(rws, WS_QX + (unsigned)bid * 1920u + (unsigned)t * 16u, r.x, r.y, tg);
  }
  FENCE();
  __builtin_amdgcn_s_barrier();
  FENCE();
  STAMP(2);
  const unsigned gq_off = WS_QX + (unsigned)(h * 8 + (lane >> 3)) * 1920u + (unsigned)w * 256u + (unsigned)(lane & 7) * 16u;
  const unsigned gq1 = w < 7 ? 128u : 0u;
#define GQ_LOAD(G) do { G[0] = ld2(rws, gq_off); G[1] = ld2(rws, gq_off + gq1); FENCE(); } while (0)
#define GQ_OK(G) __all((mism(G[0], tg) | mism(G[1], tg)) == 0u)
#define GQ_DEC(G) do { qd[0] = dec_a(G[0]); qd[1] = dec_b(G[0]); qd[2] = dec_a(G[1]); qd[3] = dec_b(G[1]); } while (0)
  v4u gqa[2];
  float qd[4];
  ISSUE(2, ubase);
  const int l40 = lane < 40 ? lane : 39;
  v4u w2_r[2];
  w2_r[0] = __builtin_nontemporal_load((const v4u*)(p.w2 + (size_t)(48 * h + hr0) * DM + 320 * chf + 8 * l40));
  w2_r[1] = __builtin_nontemporal_load((const v4u*)(p.w2 + (size_t)(48 * h + hr1) * DM + 320 * chf + 8 * l40));
  const unsigned b1a = ((const unsigned*)p.b1)[((48 * h + hr0) >> 1) + zero_v], b1b = ((const unsigned*)p.b1)[((48 * h + hr1) >> 1) + zero_v];
  FENCE();
  __builtin_amdgcn_s_barrier();
  FENCE();
  GQ_LOAD(gqa);
  __builtin_amdgcn_s_barrier();
  FENCE();
  ISSUE(3, ubase);
  GQ_DEC(gqa);
  if (!GQ_OK(gqa)) {
    STAMP(11);
#pragma unroll 1
    for (unsigned spin = 0; spin < SPIN_CAP; ++spin) {
      v4u gqb[2];
      GQ_LOAD(gqb);
      GQ_DEC(gqb);
      if (GQ_OK(gqb)) break;
      __builtin_amdgcn_s_sleep(4);
    }
  }
  FENCE();
  {
#pragma unroll
    for (int i = 0; i < 4; ++i) qd[i] = grp8_sum(qd[i]);
    if (lane < 8) {
#pragma unroll
      for (int i = 0; i < 2; ++i) {
        const int col = 32 * w + 16 * i + 2 * lane;
        if (col < 192) qsh[col >> 1] = (unsigned)f2bf(qd[2 * i]) | ((unsigned)f2bf(qd[2 * i + 1]) << 16);
        else if (col < 240) *(v2f*)(hid + col - 192) = v2f{qd[2 * i], qd[2 * i + 1]};
      }
    }
  }
  wg_barrier();
  STAMP(3);
  v4u qv[3];
#pragma unroll
  for (int j = 0; j < 3; ++j) qv[j] = *(const v4u*)(qsh + j * 32 + (lane & 7) * 4);
  const float hraw0 = hid[hr0], hraw1 = hid[hr1];

  {
    float hv[2];
#pragma unroll
    for (int i = 0; i < 2; ++i) {
      const int hr = i ? hr1 : hr0;
      const unsigned bw = i ? b1b : b1a;
      const float hh = rbf(rbf(i ? hraw1 : hraw0) + ((hr & 1) ? hi_f(bw) : lo_f(bw)));
      const float u2 = 2.f * 0.7978845608028654f * (hh + 0.044715f * hh * hh * hh);
      hv[i] = rbf(hh / (1.f + __expf(-u2)));
    }
    if (w >= 4) hv[1] = 0.f;
    float y[8];
#pragma unroll
    for (int e = 0; e < 4; ++e) {
      y[2 * e] = hv[0] * lo_f(w2_r[0][e]) + hv[1] * lo_f(w2_r[1][e]);
      y[2 * e + 1] = hv[0] * hi_f(w2_r[0][e]) + hv[1] * hi_f(w2_r[1][e]);
    }
    if (lane < 40) {
      float4* dst = (float4*)(ysh + w * 320 + 8 * lane);
      dst[0] = make_float4(y[0], y[1], y[2], y[3]); dst[1] = make_float4(y[4], y[5], y[6], y[7]);
    }
  }
  FENCE();

  v2f acc[12];
#pragma unroll
  for (int i = 0; i < 12; ++i) acc[i] = v2f{0.f, 0.f};
  float m = -1e30f, l = 0.f;
#define CONSUME(slot) do { \
    float d0 = 0.f, d1 = 0.f, d2 = 0.f; \
    _Pragma("unroll") for (int e = 0; e < 4; ++e) { d0 = dot2(kb[slot][0][e], qv[0][e], d0); d1 = dot2(kb[slot][1][e], qv[1][e], d1); d2 = dot2(kb[slot][2][e], qv[2][e], d2); } \
    float dsum = (d0 + d1) + d2; \
    dsum += dpp<0xB1>(dsum); dsum += dpp<0x4E>(dsum); dsum += dpp<0x141>(dsum); \
    const float tt_ = rbf(rbf(dsum) * SCALE) * LOG2E; \
    const float mn = fmaxf(m, tt_); \
    const float al = __builtin_amdgcn_exp2f(m - mn), pp = __builtin_amdgcn_exp2f(tt_ - mn); \
    l = l * al + pp; m = mn; \
    const v2f al2 = v2f{al, al}, pp2 = v2f{pp, pp}; \
    _Pragma("unroll") for (int jj = 0; jj < 3; ++jj) _Pragma("unroll") for (int e = 0; e < 4; ++e) { \
      const unsigned u_ = vb[slot][jj][e]; \
      acc[jj * 4 + e] = __builtin_elementwise_fma(pp2, v2f{lo_f(u_), hi_f(u_)}, acc[jj * 4 + e] * al2); } \
    asm volatile("" : "+v"(acc[0]), "+v"(acc[1]), "+v"(acc[2]), "+v"(acc[3]), "+v"(acc[4]), "+v"(acc[5]), "+v"(acc[6]), "+v"(acc[7]), \
                      "+v"(acc[8]), "+v"(acc[9]), "+v"(acc[10]), "+v"(acc[11]), "+v"(m), "+v"(l) :: "memory"); \
    __builtin_amdgcn_sched_barrier(0); } while (0)
#define WAVE_MERGE(buf) do { \
    const float M_ = grp8_max(m); \
    const float sc_ = __builtin_amdgcn_exp2f(m - M_); \
    const float l_ = grp8_sum(l * sc_); \
    float a_[24], b_[12], c_[6]; \
    _Pragma("unroll") for (int i = 0; i < 12; ++i) { a_[2 * i] = acc[i].x * sc_; a_[2 * i + 1] = acc[i].y * sc_; } \
    _Pragma("unroll") for (int i = 0; i < 12; ++i) b_[i] = sw32(a_[i], a_[i + 12]); \
    _Pragma("unroll") for (int i = 0; i < 6; ++i) c_[i] = sw16(b_[i], b_[i + 6]); \
    _Pragma("unroll") for (int i = 0; i < 6; ++i) c_[i] += dpp<0x128>(c_[i]); \
    float* mb_ = mrg + (buf) * 1600 + w * 200; \
    if ((lane & 8) == 0) { v2f* dst_ = (v2f*)(mb_ + ((lane >> 4) * 8 + (lane & 7)) * 6); \
      dst_[0] = v2f{c_[0], c_[1]}; dst_[1] = v2f{c_[2], c_[3]}; dst_[2] = v2f{c_[4], c_[5]}; } \
    if (lane == 0) *(v2f*)(mb_ + 192) = v2f{M_, l_}; } while (0)
  const int pa = 8 * (t >> 5) + 2 * (t & 3), pc8 = (t >> 2) & 7;
  const int ppos = t < 96 ? ((pa / 6) * 8 + pc8) * 6 + pa % 6 : 192;
#define PUBLISH(active, buf, rec) do { \
    v2f r_ = v2f{0.f, 0.f}; unsigned off_ = OOR; \
    if ((active) && t < 97) { \
      const float* mb_ = mrg + (buf) * 1600; \
      float mw_[NW]; float MM_ = -1e30f; \
      _Pragma("unroll") for (int i = 0; i < NW; ++i) { mw_[i] = mb_[i * 200 + 192]; MM_ = fmaxf(MM_, mw_[i]); } \
      MM_ = q24(MM_);     \
      _Pragma("unroll") for (int i = 0; i < NW; ++i) { const float e_ = __builtin_amdgcn_exp2f(mw_[i] - MM_); r_ += v2f{e_, e_} * *(const v2f*)(mb_ + i * 200 + ppos); } \
      if (t == 96) r_.x = MM_; \
      off_ = t < 96 ? WS_PART + (unsigned)(h * NREC + (rec)) * 1536u + (unsigned)t * 16u : WS_CTL + (unsigned)h * CTL_STR + (unsigned)(rec) * 16u; } \
    st2(rws, off_, r_.x, r_.y, tg); FENCE(); } while (0)

  const __amdgpu_buffer_rsrc_t rc = __builtin_amdgcn_make_buffer_rsrc((void*)(p.ws + WS_CTL + (size_t)h * CTL_STR + 1280), 0, 4, 0x00020000);
  typedef __attribute__((address_space(3))) unsigned lu32;
  volatile lu32* nx = (volatile lu32*)(cnt + 2);
  unsigned nxt = (unsigned)(s + NSPLIT);
  int it = 0, rec_cur = s, rec_pend = 0;
  bool pend = false;
  while (nxt < (unsigned)NSC) {
    const bool dyn = it + 2 >= NSTAT;
    const unsigned tk = __builtin_amdgcn_raw_ptr_buffer_atomic_add_i32(1, rc, (dyn && t == 0) ? 0u : 0x100u, 0, 16);
    FENCE();
    PUBLISH(pend, (it + 1) & 1, rec_pend);
    const unsigned base = (nxt * NW + (unsigned)w) * (RING * 3072u);
#pragma unroll
    for (int d = 0; d < RING; ++d) { CONSUME(d); ISSUE(d, base); }
    pend = it >= NSTAT - 1;
    if (pend) {
      WAVE_MERGE(it & 1);
#pragma unroll
      for (int i = 0; i < 12; ++i) acc[i] = v2f{0.f, 0.f};
      m = -1e30f; l = 0.f;
      rec_pend = rec_cur;
    }
    const unsigned tks = __builtin_amdgcn_readfirstlane(tk);
    if (t == 0) nx[it & 1] = dyn ? tks + (unsigned)(NSPLIT * NSTAT) : (unsigned)(s + NSPLIT * (it + 2));
    wg_barrier();
    FENCE();
    rec_cur = it + 1 >= NSTAT ? NSPLIT + (int)nxt - NSPLIT * NSTAT : s;
    nxt = __builtin_amdgcn_readfirstlane(nx[it & 1]);
    ++it;
  }
  PUBLISH(pend, (it + 1) & 1, rec_pend);
  const int fb = bid < 40 ? bid : 0;
  unsigned xfin, b2fin;
  v4u wo_r[6];
#define LOAD_WO() do { \
    xfin = ((const unsigned*)p.x)[8 * fb + (lane & 7)]; b2fin = ((const unsigned*)p.b2)[8 * fb + (lane & 7)]; \
    _Pragma("unroll") for (int i = 0; i < 6; ++i) wo_r[i] = __builtin_nontemporal_load((const v4u*)(p.wo + (size_t)(DH * h + 48 * rq + 6 * w + i) * DM + 320 * chf + 8 * l40)); \
    FENCE(); } while (0)
#define HINT() do { unsigned c_ = 0; \
    if (lane == 0) c_ = __hip_atomic_fetch_add(cnt, 1u, __ATOMIC_RELAXED, __HIP_MEMORY_SCOPE_WORKGROUP); \
    c_ = __builtin_amdgcn_readfirstlane(c_); \
    st2(rws, (c_ == NW - 1 && lane == 0) ? WS_CTL + (unsigned)h * CTL_STR + 1024u + (unsigned)s * 16u : OOR, 0.f, 0.f, tg); \
    FENCE(); } while (0)
  if (HINT_AT == 0) HINT();
  CONSUME(0);
  if (HINT_AT == 1) HINT();
  CONSUME(1);
  if (HINT_AT == 2) HINT();
  LOAD_WO();
  CONSUME(2);
  if (HINT_AT == 3) HINT();
  CONSUME(3);
  STAMP(4);

  WAVE_MERGE(it & 1);
  wg_barrier();
  STAMP(5);
  PUBLISH(true, it & 1, rec_cur);
  if (t >= 128 && t < 288) {
    const int ty = t - 128;
    v2f r = v2f{0.f, 0.f};
#pragma unroll
    for (int i = 0; i < NW; ++i) r += *(const v2f*)(ysh + i * 320 + 2 * ty);
    st2(rws, WS_SP + (unsigned)(256 + bid) * 2560u + (unsigned)ty * 16u, r.x, r.y, tg);
  }
  FENCE();
  STAMP(6);
  v2f wf[6][4];
#pragma unroll
  for (int i = 0; i < 6; ++i)
#pragma unroll
    for (int e = 0; e < 4; ++e) wf[i][e] = v2f{lo_f(wo_r[i][e]), hi_f(wo_r[i][e])};
#pragma unroll
  for (int i = 0; i < 6; ++i) asm volatile("" : "+v"(wf[i][0]), "+v"(wf[i][1]), "+v"(wf[i][2]), "+v"(wf[i][3]));
  FENCE();
  if (w == NW - 1) {
    const unsigned cbase = WS_CTL + (unsigned)h * CTL_STR;
#pragma unroll 1
    for (unsigned spin = 0; spin < SPIN_CAP; ++spin) {
      const v4u g = ld2(rws, cbase + 1024u + (unsigned)(lane & 7) * 16u);
      if (__all(mism(g, tg) == 0u)) break;
      __builtin_amdgcn_s_sleep(HINT_SLEEP);
    }
    STAMP(7);
    unsigned po[NRND], mo[NRND];
    float ar[NRND][6], ms[NRND], ls[NRND];
#pragma unroll
    for (int r = 0; r < NRND; ++r) {
      po[r] = WS_PART + (unsigned)(h * NREC + 8 * r + (lane >> 3)) * 1536u + (unsigned)rq * 384u + (unsigned)(lane & 7) * 16u;
      mo[r] = cbase + (unsigned)(8 * r + (lane >> 3)) * 16u;
      ms[r] = 0.f; ls[r] = 1.f;
#pragma unroll
      for (int i = 0; i < 6; ++i) ar[r][i] = 0.f;
    }
#pragma unroll 1
    for (unsigned spin = 0; spin < SPIN_CAP; ++spin) {
      v4u g[NRND][4];
#pragma unroll
      for (int r = 0; r < NRND; ++r) { g[r][0] = ld2(rws, po[r]); g[r][1] = ld2(rws, po[r] + 128u); g[r][2] = ld2(rws, po[r] + 256u); g[r][3] = ld2(rws, mo[r]); }
      unsigned all_po = OOR;
#pragma unroll
      for (int r = 0; r < NRND; ++r) {
        const bool got = ((mism(g[r][0], tg) | mism(g[r][1], tg)) | (mism(g[r][2], tg) | mism(g[r][3], tg))) == 0u;
        if (got) {
          ar[r][0] = dec_a(g[r][0]); ar[r][1] = dec_b(g[r][0]); ar[r][2] = dec_a(g[r][1]); ar[r][3] = dec_b(g[r][1]); ar[r][4] = dec_a(g[r][2]); ar[r][5] = dec_b(g[r][2]);
          ms[r] = dec_a(g[r][3]); ls[r] = dec_b(g[r][3]); po[r] = OOR; mo[r] = OOR;
        }
        all_po &= po[r];
      }
      if (__all(all_po == OOR)) break;
      __builtin_amdgcn_s_sleep(POLL_SLEEP);
    }
    float MM = ms[0];
#pragma unroll
    for (int r = 1; r < NRND; ++r) MM = fmaxf(MM, ms[r]);
    MM = grp8_max(MM);
    float er[NRND], Ls = 0.f;
#pragma unroll
    for (int r = 0; r < NRND; ++r) { er[r] = __builtin_amdgcn_exp2f(ms[r] - MM); Ls += er[r] * ls[r]; }
    const float inv = 1.f / grp8_sum(Ls);
    float av[6];
#pragma unroll
    for (int i = 0; i < 6; ++i) {
      float v = 0.f;
#pragma unroll
      for (int r = 0; r < NRND; ++r) v += er[r] * ar[r][i];
      av[i] = rbf(grp8_sum(v) * inv);
    }
    if (lane < 8) {
#pragma unroll
      for (int i = 0; i < 3; ++i) *(v2f*)(osh + 16 * i + 2 * lane) = v2f{av[2 * i], av[2 * i + 1]};
    }
  }
  FENCE();
  wg_barrier();
  STAMP(8);
  {
    const v2f o01 = *(const v2f*)(osh + 6 * w), o23 = *(const v2f*)(osh + 6 * w + 2), o45 = *(const v2f*)(osh + 6 * w + 4);
    v2f a[4];
#pragma unroll
    for (int e = 0; e < 4; ++e) {
      const v2f u0 = __builtin_elementwise_fma(v2f{o01.y, o01.y}, wf[1][e], v2f{o01.x, o01.x} * wf[0][e]);
      const v2f u1 = __builtin_elementwise_fma(v2f{o23.y, o23.y}, wf[3][e], v2f{o23.x, o23.x} * wf[2][e]);
      const v2f u2 = __builtin_elementwise_fma(v2f{o45.y, o45.y}, wf[5][e], v2f{o45.x, o45.x} * wf[4][e]);
      a[e] = (u0 + u1) + u2;
    }
    if (lane < 40) {
      float4* dst = (float4*)(ssh + w * 320 + 8 * lane);
      dst[0] = make_float4(a[0].x, a[0].y, a[1].x, a[1].y); dst[1] = make_float4(a[2].x, a[2].y, a[3].x, a[3].y);
    }
  }
  wg_barrier();
  if (t < 160) {
    v2f r = v2f{0.f, 0.f};
#pragma unroll
    for (int i = 0; i < NW; ++i) r += *(const v2f*)(ssh + i * 320 + 2 * t);
    st2(rws, WS_SP + (unsigned)bid * 2560u + (unsigned)t * 16u, r.x, r.y, tg);
  }
  STAMP(9);
  if (bid < 40) {
    const int fch = fb >= 20 ? 1 : 0;
    const unsigned coff = (unsigned)(16 * fb - 320 * fch) * 8u + (unsigned)(lane & 7) * 16u;
    unsigned so[4];
#pragma unroll
    for (int i = 0; i < 4; ++i) so[i] = WS_SP + (unsigned)((i >> 1) * 256 + 2 * (16 * w + 8 * (i & 1) + (lane >> 3)) + fch) * 2560u + coff;
    float va[4] = {0.f, 0.f, 0.f, 0.f}, vb2[4] = {0.f, 0.f, 0.f, 0.f};
#pragma unroll 1
    for (unsigned spin = 0; spin < SPIN_CAP; ++spin) {
      v4u g[4];
#pragma unroll
      for (int i = 0; i < 4; ++i) g[i] = ld2(rws, so[i]);
#pragma unroll
      for (int i = 0; i < 4; ++i) if (mism(g[i], tg) == 0u) { va[i] = dec_a(g[i]); vb2[i] = dec_b(g[i]); so[i] = OOR; }
      if (__all(((so[0] & so[1]) & (so[2] & so[3])) == OOR)) break;
      if (__all((so[2] & so[3]) == OOR)) __builtin_amdgcn_s_sleep(POLL_SLEEP); else __builtin_amdgcn_s_sleep(FIN_SLEEP);
    }
    const float s0 = grp8_sum(va[0] + va[1]), s1 = grp8_sum(vb2[0] + vb2[1]);
    const float y0 = grp8_sum(va[2] + va[3]), y1 = grp8_sum(vb2[2] + vb2[3]);
    if (lane < 8) *(float4*)(fin + w * 32 + 4 * lane) = make_float4(s0, s1, y0, y1);
    wg_barrier();
    if (t < 8) {
      float4 r = make_float4(0.f, 0.f, 0.f, 0.f);
#pragma unroll
      for (int i = 0; i < NW; ++i) { const float4 v = *(const float4*)(fin + i * 32 + 4 * t); r.x += v.x; r.y += v.y; r.z += v.z; r.w += v.w; }
      const float ya = rbf(rbf(r.z) + lo_f(b2fin)), yb = rbf(rbf(r.w) + hi_f(b2fin));
      const float ga = rbf(ya / (1.f + __expf(-ya))), gb = rbf(yb / (1.f + __expf(-yb)));
      const unsigned res = (unsigned)f2bf(lo_f(xfin) + rbf(rbf(r.x) * ga)) | ((unsigned)f2bf(hi_f(xfin) + rbf(rbf(r.y) * gb)) << 16);
      ((unsigned*)p.out)[8 * fb + t] = res;
      if (bid == 0 && t == 0) __hip_atomic_store((u64*)(p.ws + WS_EPOCH), Enew, __ATOMIC_RELAXED, __HIP_MEMORY_SCOPE_AGENT);
    }
  }
  STAMP(10);
}

extern "C" void kernel_launch(void* const* d_in, const int* in_sizes, int n_in, void* d_out, int out_size, void* d_ws, size_t ws_size, hipStream_t stream) {
  Params p;
  p.x = (const unsigned short*)d_in[0];
  p.K = (const unsigned char*)d_in[1];
  p.V = (const unsigned char*)d_in[2];
  p.wq = (const unsigned short*)d_in[3];
  p.wo = (const unsigned short*)d_in[4];
  p.w1 = (const unsigned short*)d_in[6];
  p.b1 = (const unsigned short*)d_in[7];
  p.w2 = (const unsigned short*)d_in[8];
  p.b2 = (const unsigned short*)d_in[9];
  p.ws = (unsigned char*)d_ws;
  p.out = (unsigned short*)d_out;
  hipLaunchKernelGGL(fused_kernel, dim3(256), dim3(NT), 0, stream, p);
}
```

```cpp
#include <hip/hip_runtime.h>
#include <hip/hip_bf16.h>
#include <stdint.h>

typedef unsigned v4u __attribute__((ext_vector_type(4)));
typedef unsigned v2u __attribute__((ext_vector_type(2)));
typedef float v2f __attribute__((ext_vector_type(2)));
typedef __bf16 bf16x2 __attribute__((ext_vector_type(2)));
typedef unsigned long long u64;

constexpr int DM = 640, NH = 32, SK = 32768, DH = 192, FF = 1536;
constexpr int NSPLIT = 8, NW = 8, NT = 512, RING = 4;
constexpr size_t HEAD_BYTES = (size_t)SK * DH * 2;
constexpr float SCALE = 0.07216878364870322f;
constexpr float LOG2E = 1.4426950408889634f;
constexpr unsigned SPIN_CAP = 400000u;
constexpr unsigned OOR = 0x7ffff000u;
constexpr int HINT_SLEEP = 16, POLL_SLEEP = 1, FIN_SLEEP = 12;
constexpr int NSTAT = 14;
constexpr int NSC = SK / 256;
constexpr int NREC = NSPLIT + NSC - NSPLIT * NSTAT;
constexpr int NRND = NREC / 8;
static_assert(NREC % 8 == 0 && NREC * 16 <= 1024, "records");

constexpr unsigned WS_EPOCH = 0;
constexpr unsigned WS_CTL = 4096;
constexpr unsigned CTL_STR = 4352;
constexpr unsigned WS_QX = WS_CTL + 32 * CTL_STR;
constexpr unsigned WS_PART = WS_QX + 256 * 1920;
constexpr unsigned WS_SP = WS_PART + 32 * NREC * 1536;
constexpr unsigned WS_END = WS_SP + 2 * 256 * 2560;

__device__ __forceinline__ float rbf(float x) { return (float)(__bf16)x; }
__device__ __forceinline__ unsigned short f2bf(float x) { return __builtin_bit_cast(unsigned short, (__bf16)x); }
__device__ __forceinline__ float lo_f(unsigned u) { return __builtin_bit_cast(float, u << 16); }
__device__ __forceinline__ float hi_f(unsigned u) { return __builtin_bit_cast(float, u & 0xffff0000u); }
__device__ __forceinline__ float dot2(unsigned a, unsigned b, float c) {
  return __builtin_amdgcn_fdot2_f32_bf16(__builtin_bit_cast(bf16x2, a), __builtin_bit_cast(bf16x2, b), c, false);
}
template <int CTRL> __device__ __forceinline__ float dpp(float x) {
  return __builtin_bit_cast(float, __builtin_amdgcn_mov_dpp(__builtin_bit_cast(int, x), CTRL, 0xf, 0xf, true));
}
__device__ __forceinline__ float sw16(float a, float b) {
  auto s = __builtin_amdgcn_permlane16_swap(__float_as_uint(a), __float_as_uint(b), false, false);
  return __uint_as_float(s[0]) + __uint_as_float(s[1]);
}
__device__ __forceinline__ float sw32(float a, float b) {
  auto s = __builtin_amdgcn_permlane32_swap(__float_as_uint(a), __float_as_uint(b), false, false);
  return __uint_as_float(s[0]) + __uint_as_float(s[1]);
}
__device__ __forceinline__ float x16_sum(float x) { return sw16(x, x); }
__device__ __forceinline__ float x32_sum(float x) { return sw32(x, x); }
__device__ __forceinline__ float x16_max(float x) {
  auto s = __builtin_amdgcn_permlane16_swap(__float_as_uint(x), __float_as_uint(x), false, false);
  return fmaxf(__uint_as_float(s[0]), __uint_as_float(s[1]));
}
__device__ __forceinline__ float x32_max(float x) {
  auto s = __builtin_amdgcn_permlane32_swap(__float_as_uint(x), __float_as_uint(x), false, false);
  return fmaxf(__uint_as_float(s[0]), __uint_as_float(s[1]));
}
__device__ __forceinline__ float sum16(float x) {
  x += dpp<0xB1>(x); x += dpp<0x4E>(x); x += dpp<0x141>(x); x += dpp<0x140>(x);
  return x;
}
__device__ __forceinline__ float wave_sum(float x) { return x32_sum(x16_sum(sum16(x))); }
__device__ __forceinline__ float grp8_sum(float x) {
  x += dpp<0x128>(x);
  return x32_sum(x16_sum(x));
}
__device__ __forceinline__ float grp8_max(float x) {
  x = fmaxf(x, dpp<0x128>(x));
  return x32_max(x16_max(x));
}
__device__ __forceinline__ void wg_barrier() {
  asm volatile("s_waitcnt lgkmcnt(0)" ::: "memory");
  __builtin_amdgcn_s_barrier();
  asm volatile("" ::: "memory");
}

struct Tag { unsigned t0, t1; };
template <int TB> __device__ __forceinline__ unsigned fpack(float v) { return (__builtin_bit_cast(unsigned, v) + (1u << (TB - 1))) >> TB; }
__device__ __forceinline__ float q24(float v) { return __builtin_bit_cast(float, fpack<8>(v) << 8); }
template <int TB> __device__ __forceinline__ unsigned mism(v4u g, Tag tg) {
  return ((g.y ^ tg.t1) | (g.w ^ tg.t1)) | ((((g.x ^ tg.t0) | (g.z ^ tg.t0))) & ~(0xffffffffu >> TB));
}
template <int TB> __device__ __forceinline__ float dec_a(v4u g) { return __builtin_bit_cast(float, g.x << TB); }
template <int TB> __device__ __forceinline__ float dec_b(v4u g) { return __builtin_bit_cast(float, g.z << TB); }
template <int TB> __device__ __forceinline__ void st2(__amdgpu_buffer_rsrc_t rs, unsigned off, float a, float b, Tag tg) {
  const unsigned th = tg.t0 & ~(0xffffffffu >> TB);
  __builtin_amdgcn_raw_buffer_store_b128(v4u{th | fpack<TB>(a), tg.t1, th | fpack<TB>(b), tg.t1}, rs, off, 0, 16);
}
__device__ __forceinline__ v4u ld2(__amdgpu_buffer_rsrc_t rs, unsigned off) {
  return __builtin_amdgcn_raw_buffer_load_b128(rs, off, 0, 16);
}

struct Params {
  const unsigned short* x; const unsigned char* K; const unsigned char* V; const unsigned short* wq; const unsigned short* wo;
  const unsigned short* w1; const unsigned short* b1; const unsigned short* w2; const unsigned short* b2;
  unsigned char* ws; unsigned short* out;
};

#define FENCE() do { asm volatile("" ::: "memory"); __builtin_amdgcn_sched_barrier(0); } while (0)

__global__ void __launch_bounds__(NT, 2) fused_kernel(Params p) {
  __shared__ __attribute__((aligned(16))) float lds[16 * 240 + 8 * 320 + 2 * 8 * 200 + 8 * 32 + 48 + 48 + 96 + 16];
  float* red = lds;
  float* ysh = lds;
  float* ssh = lds + 16 * 240;
  float* mrg = ssh + 8 * 320;
  float* fin = mrg + 2 * 8 * 200;
  float* osh = fin + 8 * 32;
  float* hid = osh + 48;
  unsigned* qsh = (unsigned*)(hid + 48);
  unsigned* cnt = qsh + 96;

  const int bid = (((blockIdx.x & 7) >> 2) + 2 * (blockIdx.x >> 4)) * 8 + ((blockIdx.x >> 3) & 1) * 4 + (blockIdx.x & 3);
  const int h = bid >> 3, s = bid & 7;
  const int rq = s >> 1, chf = s & 1;
  const int t = threadIdx.x, lane = t & 63, w = __builtin_amdgcn_readfirstlane(t >> 6);
  const __amdgpu_buffer_rsrc_t rws = __builtin_amdgcn_make_buffer_rsrc((void*)p.ws, 0, (int)WS_END, 0x00020000);
  const __amdgpu_buffer_rsrc_t rk = __builtin_amdgcn_make_buffer_rsrc((void*)(p.K + (size_t)h * HEAD_BYTES), 0, (int)HEAD_BYTES, 0x00020000);
  const __amdgpu_buffer_rsrc_t rv = __builtin_amdgcn_make_buffer_rsrc((void*)(p.V + (size_t)h * HEAD_BYTES), 0, (int)HEAD_BYTES, 0x00020000);

  const unsigned loff = (lane >> 3) * 384 + (lane & 7) * 16;
  const unsigned ubase = (unsigned)(s * NW + w) * (RING * 3072u);
  v4u kb[RING][3], vb[RING][3];
#define ISSUE(slot, base) do { const unsigned so_ = (base) + (slot) * 3072u; \
    _Pragma("unroll") for (int jj = 0; jj < 3; ++jj) kb[slot][jj] = __builtin_amdgcn_raw_buffer_load_b128(rk, loff + jj * 128, so_, 2); \
    _Pragma("unroll") for (int jj = 0; jj < 3; ++jj) vb[slot][jj] = __builtin_amdgcn_raw_buffer_load_b128(rv, loff + jj * 128, so_, 2); \
    FENCE(); } while (0)
  ISSUE(0, ubase);
  __builtin_amdgcn_raw_buffer_store_b32(0u, rws, (s == 0 && t == 0) ? WS_CTL + (unsigned)h * CTL_STR + 1280u : OOR, 0, 16);
  FENCE();
  unsigned xw[5];
#pragma unroll
  for (int i = 0; i < 5; ++i) xw[i] = ((const unsigned*)p.x)[lane + 64 * i];
  const int tt = t < 480 ? t : 479;
  const int rg = tt / 30, ch = tt % 30;
  unsigned xr[5];
  v4u wv[5];
  {
    const bool isq = ch < 24;
    const unsigned short* wsel = isq ? p.wq : p.w1;
    const unsigned ldw = isq ? NH * DH : FF;
    const unsigned coff = isq ? DH * h + 8 * ch : 48 * h + 8 * (ch - 24);
    const unsigned short* base = wsel + (size_t)((80 * s + rg) * ldw + coff);
#pragma unroll
    for (int i = 0; i < 5; ++i) wv[i] = __builtin_nontemporal_load((const v4u*)(base + (size_t)(i * 16 * ldw)));
#pragma unroll
    for (int i = 0; i < 5; ++i) xr[i] = ((const unsigned*)p.x)[(80 * s + rg + 16 * i) >> 1];
  }
  unsigned zero_v;
  asm volatile("v_mov_b32 %0, 0" : "=v"(zero_v));
  v2u Ev = __builtin_amdgcn_raw_buffer_load_b64(rws, WS_EPOCH + zero_v, 0, 16);
  const int hr0 = 12 * rq + w, hr1 = 12 * rq + 8 + (w & 3);
  if (t == 0) cnt[0] = 0u;
  FENCE();
  __builtin_amdgcn_s_barrier();
  FENCE();
  ISSUE(1, ubase);
  asm volatile("" : "+v"(Ev), "+v"(xw[0]), "+v"(xw[1]), "+v"(xw[2]), "+v"(xw[3]), "+v"(xw[4]) :: "memory");
  __builtin_amdgcn_sched_barrier(0);
  Tag tg;
  u64 Enew;
  {
    const u64 E = ((u64)__builtin_amdgcn_readfirstlane(Ev.y) << 32) | (unsigned)__builtin_amdgcn_readfirstlane(Ev.x);
    u64 z = E + 0x9E3779B97F4A7C15ull;
    z = (z ^ (z >> 30)) * 0xBF58476D1CE4E5B9ull; z = (z ^ (z >> 27)) * 0x94D049BB133111EBull; z ^= z >> 31;
    Enew = z; tg.t0 = (unsigned)z << 24; tg.t1 = (unsigned)(z >> 8) | 1u;
  }
  {
    float sum = 0.f;
#pragma unroll
    for (int i = 0; i < 5; ++i) sum += lo_f(xw[i]) + hi_f(xw[i]);
    const float mean = wave_sum(sum) * (1.f / DM);
    float var = 0.f;
#pragma unroll
    for (int i = 0; i < 5; ++i) { const float a = lo_f(xw[i]) - mean, b = hi_f(xw[i]) - mean; var += a * a + b * b; }
    var = wave_sum(var) * (1.f / DM);
    const float mb = rbf(mean), den = rbf(sqrtf(rbf(rbf(var) + 1e-5f)));
    float a8[8] = {0.f, 0.f, 0.f, 0.f, 0.f, 0.f, 0.f, 0.f};
#pragma unroll
    for (int i = 0; i < 5; ++i) {
      const float xe = (rg & 1) ? hi_f(xr[i]) : lo_f(xr[i]);
      const float xn = rbf(rbf(xe - mb) / den);
#pragma unroll
      for (int e = 0; e < 4; ++e) { a8[2 * e] += xn * lo_f(wv[i][e]); a8[2 * e + 1] += xn * hi_f(wv[i][e]); }
    }
    if (t < 480) {
      float4* dst = (float4*)(red + rg * 240 + ch * 8);
      dst[0] = make_float4(a8[0], a8[1], a8[2], a8[3]); dst[1] = make_float4(a8[4], a8[5], a8[6], a8[7]);
    }
  }
  wg_barrier();
  if (t < 120) {
    v2f r = v2f{0.f, 0.f};
#pragma unroll
    for (int i = 0; i < 16; ++i) r += *(const v2f*)(red + i * 240 + 2 * t);
    st2<4>(rws, WS_QX + (unsigned)bid * 1920u + (unsigned)t * 16u, r.x, r.y, tg);
  }
  FENCE();
  __builtin_amdgcn_s_barrier();
  FENCE();
  const unsigned gq_off = WS_QX + (unsigned)(h * 8 + (lane >> 3)) * 1920u + (unsigned)w * 256u + (unsigned)(lane & 7) * 16u;
  const unsigned gq1 = w < 7 ? 128u : 0u;
#define GQ_LOAD(G) do { G[0] = ld2(rws, gq_off); G[1] = ld2(rws, gq_off + gq1); FENCE(); } while (0)
#define GQ_OK(G) __all((mism<4>(G[0], tg) | mism<4>(G[1], tg)) == 0u)
#define GQ_DEC(G) do { qd[0] = dec_a<4>(G[0]); qd[1] = dec_b<4>(G[0]); qd[2] = dec_a<4>(G[1]); qd[3] = dec_b<4>(G[1]); } while (0)
  v4u gqa[2];
  float qd[4];
  ISSUE(2, ubase);
  const int l40 = lane < 40 ? lane : 39;
  v4u w2_r[2];
  w2_r[0] = __builtin_nontemporal_load((const v4u*)(p.w2 + (size_t)(48 * h + hr0) * DM + 320 * chf + 8 * l40));
  w2_r[1] = __builtin_nontemporal_load((const v4u*)(p.w2 + (size_t)(48 * h + hr1) * DM + 320 * chf + 8 * l40));
  const unsigned b1a = ((const unsigned*)p.b1)[((48 * h + hr0) >> 1) + zero_v], b1b = ((const unsigned*)p.b1)[((48 * h + hr1) >> 1) + zero_v];
  FENCE();
  __builtin_amdgcn_s_barrier();
  FENCE();
  GQ_LOAD(gqa);
  __builtin_amdgcn_s_barrier();
  FENCE();
  ISSUE(3, ubase);
  GQ_DEC(gqa);
  if (!GQ_OK(gqa)) {
#pragma unroll 1
    for (unsigned spin = 0; spin < SPIN_CAP; ++spin) {
      v4u gqb[2];
      GQ_LOAD(gqb);
      GQ_DEC(gqb);
      if (GQ_OK(gqb)) break;
      __builtin_amdgcn_s_sleep(4);
    }
  }
  FENCE();
  {
#pragma unroll
    for (int i = 0; i < 4; ++i) qd[i] = grp8_sum(qd[i]);
    if (lane < 8) {
#pragma unroll
      for (int i = 0; i < 2; ++i) {
        const int col = 32 * w + 16 * i + 2 * lane;
        if (col < 192) qsh[col >> 1] = (unsigned)f2bf(qd[2 * i]) | ((unsigned)f2bf(qd[2 * i + 1]) << 16);
        else if (col < 240) *(v2f*)(hid + col - 192) = v2f{qd[2 * i], qd[2 * i + 1]};
      }
    }
  }
  wg_barrier();
  v4u qv[3];
#pragma unroll
  for (int j = 0; j < 3; ++j) qv[j] = *(const v4u*)(qsh + j * 32 + (lane & 7) * 4);
  const float hraw0 = hid[hr0], hraw1 = hid[hr1];
  {
    float hv[2];
#pragma unroll
    for (int i = 0; i < 2; ++i) {
      const int hr = i ? hr1 : hr0;
      const unsigned bw = i ? b1b : b1a;
      const float hh = rbf(rbf(i ? hraw1 : hraw0) + ((hr & 1) ? hi_f(bw) : lo_f(bw)));
      const float u2 = 2.f * 0.7978845608028654f * (hh + 0.044715f * hh * hh * hh);
      hv[i] = rbf(hh / (1.f + __expf(-u2)));
    }
    if (w >= 4) hv[1] = 0.f;
    float y[8];
#pragma unroll
    for (int e = 0; e < 4; ++e) {
      y[2 * e] = hv[0] * lo_f(w2_r[0][e]) + hv[1] * lo_f(w2_r[1][e]);
      y[2 * e + 1] = hv[0] * hi_f(w2_r[0][e]) + hv[1] * hi_f(w2_r[1][e]);
    }
    if (lane < 40) {
      float4* dst = (float4*)(ysh + w * 320 + 8 * lane);
      dst[0] = make_float4(y[0], y[1], y[2], y[3]); dst[1] = make_float4(y[4], y[5], y[6], y[7]);
    }
  }
  FENCE();

  v2f acc[12];
#pragma unroll
  for (int i = 0; i < 12; ++i) acc[i] = v2f{0.f, 0.f};
  float m = -1e30f, l = 0.f;
#define CONSUME(slot) do { \
    float d0 = 0.f, d1 = 0.f, d2 = 0.f; \
    _Pragma("unroll") for (int e = 0; e < 4; ++e) { d0 = dot2(kb[slot][0][e], qv[0][e], d0); d1 = dot2(kb[slot][1][e], qv[1][e], d1); d2 = dot2(kb[slot][2][e], qv[2][e], d2); } \
    float dsum = (d0 + d1) + d2; \
    dsum += dpp<0xB1>(dsum); dsum += dpp<0x4E>(dsum); dsum += dpp<0x141>(dsum); \
    const float tt_ = rbf(rbf(dsum) * SCALE) * LOG2E; \
    const float mn = fmaxf(m, tt_); \
    const float al = __builtin_amdgcn_exp2f(m - mn), pp = __builtin_amdgcn_exp2f(tt_ - mn); \
    l = l * al + pp; m = mn; \
    const v2f al2 = v2f{al, al}, pp2 = v2f{pp, pp}; \
    _Pragma("unroll") for (int jj = 0; jj < 3; ++jj) _Pragma("unroll") for (int e = 0; e < 4; ++e) { \
      const unsigned u_ = vb[slot][jj][e]; \
      acc[jj * 4 + e] = __builtin_elementwise_fma(pp2, v2f{lo_f(u_), hi_f(u_)}, acc[jj * 4 + e] * al2); } \
    asm volatile("" : "+v"(acc[0]), "+v"(acc[1]), "+v"(acc[2]), "+v"(acc[3]), "+v"(acc[4]), "+v"(acc[5]), "+v"(acc[6]), "+v"(acc[7]), \
                      "+v"(acc[8]), "+v"(acc[9]), "+v"(acc[10]), "+v"(acc[11]), "+v"(m), "+v"(l) :: "memory"); \
    __builtin_amdgcn_sched_barrier(0); } while (0)
#define WAVE_MERGE(buf) do { \
    const float M_ = grp8_max(m); \
    const float sc_ = __builtin_amdgcn_exp2f(m - M_); \
    const float l_ = grp8_sum(l * sc_); \
    float a_[24], b_[12], c_[6]; \
    _Pragma("unroll") for (int i = 0; i < 12; ++i) { a_[2 * i] = acc[i].x * sc_; a_[2 * i + 1] = acc[i].y * sc_; } \
    _Pragma("unroll") for (int i = 0; i < 12; ++i) b_[i] = sw32(a_[i], a_[i + 12]); \
    _Pragma("unroll") for (int i = 0; i < 6; ++i) c_[i] = sw16(b_[i], b_[i + 6]); \
    _Pragma("unroll") for (int i = 0; i < 6; ++i) c_[i] += dpp<0x128>(c_[i]); \
    float* mb_ = mrg + (buf) * 1600 + w * 200; \
    if ((lane & 8) == 0) { v2f* dst_ = (v2f*)(mb_ + ((lane >> 4) * 8 + (lane & 7)) * 6); \
      dst_[0] = v2f{c_[0], c_[1]}; dst_[1] = v2f{c_[2], c_[3]}; dst_[2] = v2f{c_[4], c_[5]}; } \
    if (lane == 0) *(v2f*)(mb_ + 192) = v2f{M_, l_}; } while (0)
  const int pa = 8 * (t >> 5) + 2 * (t & 3), pc8 = (t >> 2) & 7;
  const int ppos = t < 96 ? ((pa / 6) * 8 + pc8) * 6 + pa % 6 : 192;
#define PUBLISH(active, buf, rec) do { \
    v2f r_ = v2f{0.f, 0.f}; unsigned off_ = OOR; \
    if ((active) && t < 97) { \
      const float* mb_ = mrg + (buf) * 1600; \
      float mw_[NW]; float MM_ = -1e30f; \
      _Pragma("unroll") for (int i = 0; i < NW; ++i) { mw_[i] = mb_[i * 200 + 192]; MM_ = fmaxf(MM_, mw_[i]); } \
      MM_ = q24(MM_);     \
      _Pragma("unroll") for (int i = 0; i < NW; ++i) { const float e_ = __builtin_amdgcn_exp2f(mw_[i] - MM_); r_ += v2f{e_, e_} * *(const v2f*)(mb_ + i * 200 + ppos); } \
      if (t == 96) r_.x = MM_; \
      off_ = t < 96 ? WS_PART + (unsigned)(h * NREC + (rec)) * 1536u + (unsigned)t * 16u : WS_CTL + (unsigned)h * CTL_STR + (unsigned)(rec) * 16u; } \
    st2<8>(rws, off_, r_.x, r_.y, tg); FENCE(); } while (0)

  const __amdgpu_buffer_rsrc_t rc = __builtin_amdgcn_make_buffer_rsrc((void*)(p.ws + WS_CTL + (size_t)h * CTL_STR + 1280), 0, 4, 0x00020000);
  typedef __attribute__((address_space(3))) unsigned lu32;
  volatile lu32* nx = (volatile lu32*)(cnt + 2);
  unsigned nxt = (unsigned)(s + NSPLIT);
  int it = 0, rec_cur = s, rec_pend = 0;
  bool pend = false;
  while (nxt < (unsigned)NSC) {
    const bool dyn = it + 2 >= NSTAT;
    const unsigned tk = __builtin_amdgcn_raw_ptr_buffer_atomic_add_i32(1, rc, (dyn && t == 0) ? 0u : 0x100u, 0, 16);
    FENCE();
    PUBLISH(pend, (it + 1) & 1, rec_pend);
    const unsigned base = (nxt * NW + (unsigned)w) * (RING * 3072u);
#pragma unroll
    for (int d = 0; d < RING; ++d) { CONSUME(d); ISSUE(d, base); }
    pend = it >= NSTAT - 1;
    if (pend) {
      WAVE_MERGE(it & 1);
#pragma unroll
      for (int i = 0; i < 12; ++i) acc[i] = v2f{0.f, 0.f};
      m = -1e30f; l = 0.f;
      rec_pend = rec_cur;
    }
    const unsigned tks = __builtin_amdgcn_readfirstlane(tk);
    if (t == 0) nx[it & 1] = dyn ? (tks < (unsigned)(NSC - NSPLIT * NSTAT) ? tks + (unsigned)(NSPLIT * NSTAT) : (unsigned)NSC) : (unsigned)(s + NSPLIT * (it + 2));
    wg_barrier();
    FENCE();
    rec_cur = it + 1 >= NSTAT ? NSPLIT + (int)nxt - NSPLIT * NSTAT : s;
    nxt = __builtin_amdgcn_readfirstlane(nx[it & 1]);
    ++it;
  }
  PUBLISH(pend, (it + 1) & 1, rec_pend);
  const int fb = bid < 40 ? bid : 0;
  CONSUME(0);
  CONSUME(1);
  {
    unsigned c_ = 0;
    if (lane == 0) c_ = __hip_atomic_fetch_add(cnt, 1u, __ATOMIC_RELAXED, __HIP_MEMORY_SCOPE_WORKGROUP);
    c_ = __builtin_amdgcn_readfirstlane(c_);
    st2<8>(rws, (c_ == NW - 1 && lane == 0) ? WS_CTL + (unsigned)h * CTL_STR + 1024u + (unsigned)s * 16u : OOR, 0.f, 0.f, tg);
    FENCE();
  }
  const unsigned xfin = ((const unsigned*)p.x)[8 * fb + (lane & 7)], b2fin = ((const unsigned*)p.b2)[8 * fb + (lane & 7)];
  v4u wo_r[6];
#pragma unroll
  for (int i = 0; i < 6; ++i) wo_r[i] = __builtin_nontemporal_load((const v4u*)(p.wo + (size_t)(DH * h + 48 * rq + 6 * w + i) * DM + 320 * chf + 8 * l40));
  FENCE();
  CONSUME(2);
  CONSUME(3);

  WAVE_MERGE(it & 1);
  wg_barrier();
  PUBLISH(true, it & 1, rec_cur);
  if (t >= 128 && t < 288) {
    const int ty = t - 128;
    v2f r = v2f{0.f, 0.f};
#pragma unroll
    for (int i = 0; i < NW; ++i) r += *(const v2f*)(ysh + i * 320 + 2 * ty);
    st2<8>(rws, WS_SP + (unsigned)(256 + bid) * 2560u + (unsigned)ty * 16u, r.x, r.y, tg);
  }
  FENCE();
  v2f wf[6][4];
#pragma unroll
  for (int i = 0; i < 6; ++i)
#pragma unroll
    for (int e = 0; e < 4; ++e) wf[i][e] = v2f{lo_f(wo_r[i][e]), hi_f(wo_r[i][e])};
#pragma unroll
  for (int i = 0; i < 6; ++i) asm volatile("" : "+v"(wf[i][0]), "+v"(wf[i][1]), "+v"(wf[i][2]), "+v"(wf[i][3]));
  FENCE();
  if (w == NW - 1) {
    const unsigned cbase = WS_CTL + (unsigned)h * CTL_STR;
#pragma unroll 1
    for (unsigned spin = 0; spin < SPIN_CAP; ++spin) {
      const v4u g = ld2(rws, cbase + 1024u + (unsigned)(lane & 7) * 16u);
      if (__all(mism<8>(g, tg) == 0u)) break;
      __builtin_amdgcn_s_sleep(HINT_SLEEP);
    }
    unsigned po[NRND], mo[NRND];
    float ar[NRND][6], ms[NRND], ls[NRND];
#pragma unroll
    for (int r = 0; r < NRND; ++r) {
      po[r] = WS_PART + (unsigned)(h * NREC + 8 * r + (lane >> 3)) * 1536u + (unsigned)rq * 384u + (unsigned)(lane & 7) * 16u;
      mo[r] = cbase + (unsigned)(8 * r + (lane >> 3)) * 16u;
      ms[r] = 0.f; ls[r] = 1.f;
#pragma unroll
      for (int i = 0; i < 6; ++i) ar[r][i] = 0.f;
    }
#pragma unroll 1
    for (unsigned spin = 0; spin < SPIN_CAP; ++spin) {
      v4u g[NRND][4];
#pragma unroll
      for (int r = 0; r < NRND; ++r) { g[r][0] = ld2(rws, po[r]); g[r][1] = ld2(rws, po[r] + 128u); g[r][2] = ld2(rws, po[r] + 256u); g[r][3] = ld2(rws, mo[r]); }
      unsigned all_po = OOR;
#pragma unroll
      for (int r = 0; r < NRND; ++r) {
        const bool got = ((mism<8>(g[r][0], tg) | mism<8>(g[r][1], tg)) | (mism<8>(g[r][2], tg) | mism<8>(g[r][3], tg))) == 0u;
        if (got) {
          ar[r][0] = dec_a<8>(g[r][0]); ar[r][1] = dec_b<8>(g[r][0]); ar[r][2] = dec_a<8>(g[r][1]);
          ar[r][3] = dec_b<8>(g[r][1]); ar[r][4] = dec_a<8>(g[r][2]); ar[r][5] = dec_b<8>(g[r][2]);
          ms[r] = dec_a<8>(g[r][3]); ls[r] = dec_b<8>(g[r][3]); po[r] = OOR; mo[r] = OOR;
        }
        all_po &= po[r];
      }
      if (__all(all_po == OOR)) break;
      __builtin_amdgcn_s_sleep(POLL_SLEEP);
    }
    float MM = ms[0];
#pragma unroll
    for (int r = 1; r < NRND; ++r) MM = fmaxf(MM, ms[r]);
    MM = grp8_max(MM);
    float er[NRND], Ls = 0.f;
#pragma unroll
    for (int r = 0; r < NRND; ++r) { er[r] = __builtin_amdgcn_exp2f(ms[r] - MM); Ls += er[r] * ls[r]; }
    const float inv = 1.f / grp8_sum(Ls);
    float av[6];
#pragma unroll
    for (int i = 0; i < 6; ++i) {
      float v = 0.f;
#pragma unroll
      for (int r = 0; r < NRND; ++r) v += er[r] * ar[r][i];
      av[i] = rbf(grp8_sum(v) * inv);
    }
    if (lane < 8) {
#pragma unroll
      for (int i = 0; i < 3; ++i) *(v2f*)(osh + 16 * i + 2 * lane) = v2f{av[2 * i], av[2 * i + 1]};
    }
  }
  FENCE();
  wg_barrier();
  {
    const v2f o01 = *(const v2f*)(osh + 6 * w), o23 = *(const v2f*)(osh + 6 * w + 2), o45 = *(const v2f*)(osh + 6 * w + 4);
    v2f a[4];
#pragma unroll
    for (int e = 0; e < 4; ++e) {
      const v2f u0 = __builtin_elementwise_fma(v2f{o01.y, o01.y}, wf[1][e], v2f{o01.x, o01.x} * wf[0][e]);
      const v2f u1 = __builtin_elementwise_fma(v2f{o23.y, o23.y}, wf[3][e], v2f{o23.x, o23.x} * wf[2][e]);
      const v2f u2 = __builtin_elementwise_fma(v2f{o45.y, o45.y}, wf[5][e], v2f{o45.x, o45.x} * wf[4][e]);
      a[e] = (u0 + u1) + u2;
    }
    if (lane < 40) {
      float4* dst = (float4*)(ssh + w * 320 + 8 * lane);
      dst[0] = make_float4(a[0].x, a[0].y, a[1].x, a[1].y); dst[1] = make_float4(a[2].x, a[2].y, a[3].x, a[3].y);
    }
  }
  wg_barrier();
  if (t < 160) {
    v2f r = v2f{0.f, 0.f};
#pragma unroll
    for (int i = 0; i < NW; ++i) r += *(const v2f*)(ssh + i * 320 + 2 * t);
    st2<8>(rws, WS_SP + (unsigned)bid * 2560u + (unsigned)t * 16u, r.x, r.y, tg);
  }
  if (bid < 40) {
    const int fch = fb >= 20 ? 1 : 0;
    const unsigned coff = (unsigned)(16 * fb - 320 * fch) * 8u + (unsigned)(lane & 7) * 16u;
    unsigned so[4];
#pragma unroll
    for (int i = 0; i < 4; ++i) so[i] = WS_SP + (unsigned)((i >> 1) * 256 + 2 * (16 * w + 8 * (i & 1) + (lane >> 3)) + fch) * 2560u + coff;
    float va[4] = {0.f, 0.f, 0.f, 0.f}, vb2[4] = {0.f, 0.f, 0.f, 0.f};
#pragma unroll 1
    for (unsigned spin = 0; spin < SPIN_CAP; ++spin) {
      v4u g[4];
#pragma unroll
      for (int i = 0; i < 4; ++i) g[i] = ld2(rws, so[i]);
#pragma unroll
      for (int i = 0; i < 4; ++i) if (mism<8>(g[i], tg) == 0u) { va[i] = dec_a<8>(g[i]); vb2[i] = dec_b<8>(g[i]); so[i] = OOR; }
      if (__all(((so[0] & so[1]) & (so[2] & so[3])) == OOR)) break;
      if (__all((so[2] & so[3]) == OOR)) __builtin_amdgcn_s_sleep(POLL_SLEEP); else __builtin_amdgcn_s_sleep(FIN_SLEEP);
    }
    const float s0 = grp8_sum(va[0] + va[1]), s1 = grp8_sum(vb2[0] + vb2[1]);
    const float y0 = grp8_sum(va[2] + va[3]), y1 = grp8_sum(vb2[2] + vb2[3]);
    if (lane < 8) *(float4*)(fin + w * 32 + 4 * lane) = make_float4(s0, s1, y0, y1);
    wg_barrier();
    if (t < 8) {
      float4 r = make_float4(0.f, 0.f, 0.f, 0.f);
#pragma unroll
      for (int i = 0; i < NW; ++i) { const float4 v = *(const float4*)(fin + i * 32 + 4 * t); r.x += v.x; r.y += v.y; r.z += v.z; r.w += v.w; }
      const float ya = rbf(rbf(r.z) + lo_f(b2fin)), yb = rbf(rbf(r.w) + hi_f(b2fin));
      const float ga = rbf(ya / (1.f + __expf(-ya))), gb = rbf(yb / (1.f + __expf(-yb)));
      const unsigned res = (unsigned)f2bf(lo_f(xfin) + rbf(rbf(r.x) * ga)) | ((unsigned)f2bf(hi_f(xfin) + rbf(rbf(r.y) * gb)) << 16);
      ((unsigned*)p.out)[8 * fb + t] = res;
      if (bid == 0 && t == 0) __hip_atomic_store((u64*)(p.ws + WS_EPOCH), Enew, __ATOMIC_RELAXED, __HIP_MEMORY_SCOPE_AGENT);
    }
  }
}

extern "C" void kernel_launch(void* const* d_in, const int* in_sizes, int n_in, void* d_out, int out_size, void* d_ws, size_t ws_size, hipStream_t stream) {
  Params p;
  p.x = (const unsigned short*)d_in[0];
  p.K = (const unsigned char*)d_in[1];
  p.V = (const unsigned char*)d_in[2];
  p.wq = (const unsigned short*)d_in[3];
  p.wo = (const unsigned short*)d_in[4];
  p.w1 = (const unsigned short*)d_in[6];
  p.b1 = (const unsigned short*)d_in[7];
  p.w2 = (const unsigned short*)d_in[8];
  p.b2 = (const unsigned short*)d_in[9];
  p.ws = (unsigned char*)d_ws;
  p.out = (unsigned short*)d_out;
  hipLaunchKernelGGL(fused_kernel, dim3(256), dim3(NT), 0, stream, p);
}
```

```cpp
#include <hip/hip_runtime.h>
#include <hip/hip_bf16.h>
#include <stdint.h>

typedef unsigned v4u __attribute__((ext_vector_type(4)));
typedef unsigned v2u __attribute__((ext_vector_type(2)));
typedef float v2f __attribute__((ext_vector_type(2)));
typedef __bf16 bf16x2 __attribute__((ext_vector_type(2)));
typedef unsigned long long u64;

constexpr int DM = 640, NH = 32, SK = 32768, DH = 192, FF = 1536;
constexpr int NSPLIT = 8, NW = 8, NT = 512, RING = 4;
constexpr size_t HEAD_BYTES = (size_t)SK * DH * 2;
constexpr float SCALE = 0.07216878364870322f;
constexpr float LOG2E = 1.4426950408889634f;
constexpr unsigned SPIN_CAP = 400000u;
constexpr unsigned OOR = 0x7ffff000u;
constexpr int HINT_SLEEP = 16, POLL_SLEEP = 1, FIN_SLEEP = 12;
constexpr int NSTAT = 14;
constexpr int NSC = SK / 256;
constexpr int NREC = NSPLIT + NSC - NSPLIT * NSTAT;
constexpr int NRND = NREC / 8;
static_assert(NREC % 8 == 0 && NREC * 16 <= 1024, "records");

constexpr unsigned WS_EPOCH = 0;
constexpr unsigned WS_CTL = 4096;
constexpr unsigned CTL_STR = 4352;
constexpr unsigned WS_QX = WS_CTL + 32 * CTL_STR;
constexpr unsigned WS_PART = WS_QX + 256 * 1920;
constexpr unsigned WS_SP = WS_PART + 32 * NREC * 1536;
constexpr unsigned WS_END = WS_SP + 2 * 256 * 2560;

__device__ __forceinline__ float rbf(float x) { return (float)(__bf16)x; }
__device__ __forceinline__ unsigned short f2bf(float x) { return __builtin_bit_cast(unsigned short, (__bf16)x); }
__device__ __forceinline__ float lo_f(unsigned u) { return __builtin_bit_cast(float, u << 16); }
__device__ __forceinline__ float hi_f(unsigned u) { return __builtin_bit_cast(float, u & 0xffff0000u); }
__device__ __forceinline__ float dot2(unsigned a, unsigned b, float c) {
  return __builtin_amdgcn_fdot2_f32_bf16(__builtin_bit_cast(bf16x2, a), __builtin_bit_cast(bf16x2, b), c, false);
}
template <int CTRL> __device__ __forceinline__ float dpp(float x) {
  return __builtin_bit_cast(float, __builtin_amdgcn_mov_dpp(__builtin_bit_cast(int, x), CTRL, 0xf, 0xf, true));
}
__device__ __forceinline__ float sw16(float a, float b) {
  auto s = __builtin_amdgcn_permlane16_swap(__float_as_uint(a), __float_as_uint(b), false, false);
  return __uint_as_float(s[0]) + __uint_as_float(s[1]);
}
__device__ __forceinline__ float sw32(float a, float b) {
  auto s = __builtin_amdgcn_permlane32_swap(__float_as_uint(a), __float_as_uint(b), false, false);
  return __uint_as_float(s[0]) + __uint_as_float(s[1]);
}
__device__ __forceinline__ float x16_sum(float x) { return sw16(x, x); }
__device__ __forceinline__ float x32_sum(float x) { return sw32(x, x); }
__device__ __forceinline__ float x16_max(float x) {
  auto s = __builtin_amdgcn_permlane16_swap(__float_as_uint(x), __float_as_uint(x), false, false);
  return fmaxf(__uint_as_float(s[0]), __uint_as_float(s[1]));
}
__device__ __forceinline__ float x32_max(float x) {
  auto s = __builtin_amdgcn_permlane32_swap(__float_as_uint(x), __float_as_uint(x), false, false);
  return fmaxf(__uint_as_float(s[0]), __uint_as_float(s[1]));
}
__device__ __forceinline__ float sum16(float x) {
  x += dpp<0xB1>(x); x += dpp<0x4E>(x); x += dpp<0x141>(x); x += dpp<0x140>(x);
  return x;
}
__device__ __forceinline__ float wave_sum(float x) { return x32_sum(x16_sum(sum16(x))); }
__device__ __forceinline__ float grp8_sum(float x) {
  x += dpp<0x128>(x);
  return x32_sum(x16_sum(x));
}
__device__ __forceinline__ float grp8_max(float x) {
  x = fmaxf(x, dpp<0x128>(x));
  return x32_max(x16_max(x));
}
__device__ __forceinline__ void wg_barrier() {
  asm volatile("s_waitcnt lgkmcnt(0)" ::: "memory");
  __builtin_amdgcn_s_barrier();
  asm volatile("" ::: "memory");
}

struct Tag { unsigned t0, t1; };
template <int TB> __device__ __forceinline__ unsigned fpack(float v) { return (__builtin_bit_cast(unsigned, v) + (1u << (TB - 1))) >> TB; }
__device__ __forceinline__ float q24(float v) { return __builtin_bit_cast(float, fpack<8>(v) << 8); }
template <int TB> __device__ __forceinline__ unsigned mism(v4u g, Tag tg) {
  return ((g.y ^ tg.t1) | (g.w ^ tg.t1)) | ((((g.x ^ tg.t0) | (g.z ^ tg.t0))) & ~(0xffffffffu >> TB));
}
template <int TB> __device__ __forceinline__ float dec_a(v4u g) { return __builtin_bit_cast(float, g.x << TB); }
template <int TB> __device__ __forceinline__ float dec_b(v4u g) { return __builtin_bit_cast(float, g.z << TB); }
template <int TB> __device__ __forceinline__ void st2(__amdgpu_buffer_rsrc_t rs, unsigned off, float a, float b, Tag tg) {
  const unsigned th = tg.t0 & ~(0xffffffffu >> TB);
  __builtin_amdgcn_raw_buffer_store_b128(v4u{th | fpack<TB>(a), tg.t1, th | fpack<TB>(b), tg.t1}, rs, off, 0, 16);
}
__device__ __forceinline__ v4u ld2(__amdgpu_buffer_rsrc_t rs, unsigned off) {
  return __builtin_amdgcn_raw_buffer_load_b128(rs, off, 0, 16);
}

struct Params {
  const unsigned short* x; const unsigned char* K; const unsigned char* V; const unsigned short* wq; const unsigned short* wo;
  const unsigned short* w1; const unsigned short* b1; const unsigned short* w2; const unsigned short* b2;
  unsigned char* ws; unsigned short* out;
};

#define FENCE() do { asm volatile("" ::: "memory"); __builtin_amdgcn_sched_barrier(0); } while (0)

__global__ void __launch_bounds__(NT, 2) fused_kernel(Params p) {
  __shared__ __attribute__((aligned(16))) float lds[16 * 240 + 8 * 320 + 2 * 8 * 200 + 8 * 32 + 48 + 48 + 96 + 16];
  float* red = lds;
  float* ysh = lds;
  float* ssh = lds + 16 * 240;
  float* mrg = ssh + 8 * 320;
  float* fin = mrg + 2 * 8 * 200;
  float* osh = fin + 8 * 32;
  float* hid = osh + 48;
  unsigned* qsh = (unsigned*)(hid + 48);
  unsigned* cnt = qsh + 96;

  const int bid = (((blockIdx.x & 7) >> 2) + 2 * (blockIdx.x >> 4)) * 8 + ((blockIdx.x >> 3) & 1) * 4 + (blockIdx.x & 3);
  const int h = bid >> 3, s = bid & 7;
  const int rq = s >> 1, chf = s & 1;
  const int t = threadIdx.x, lane = t & 63, w = __builtin_amdgcn_readfirstlane(t >> 6);
  const __amdgpu_buffer_rsrc_t rws = __builtin_amdgcn_make_buffer_rsrc((void*)p.ws, 0, (int)WS_END, 0x00020000);
  const __amdgpu_buffer_rsrc_t rk = __builtin_amdgcn_make_buffer_rsrc((void*)(p.K + (size_t)h * HEAD_BYTES), 0, (int)HEAD_BYTES, 0x00020000);
  const __amdgpu_buffer_rsrc_t rv = __builtin_amdgcn_make_buffer_rsrc((void*)(p.V + (size_t)h * HEAD_BYTES), 0, (int)HEAD_BYTES, 0x00020000);

  const unsigned loff = (lane >> 3) * 384 + (lane & 7) * 16;
  const unsigned ubase = (unsigned)(s * NW + w) * (RING * 3072u);
  v4u kb[RING][3], vb[RING][3];
#define ISSUE(slot, base) do { const unsigned so_ = (base) + (slot) * 3072u; \
    _Pragma("unroll") for (int jj = 0; jj < 3; ++jj) kb[slot][jj] = __builtin_amdgcn_raw_buffer_load_b128(rk, loff + jj * 128, so_, 2); \
    _Pragma("unroll") for (int jj = 0; jj < 3; ++jj) vb[slot][jj] = __builtin_amdgcn_raw_buffer_load_b128(rv, loff + jj * 128, so_, 2); \
    FENCE(); } while (0)
  ISSUE(0, ubase);
  __builtin_amdgcn_raw_buffer_store_b32(0u, rws, (s == 0 && t == 0) ? WS_CTL + (unsigned)h * CTL_STR + 1280u : OOR, 0, 16);
  FENCE();
  unsigned xw[5];
#pragma unroll
  for (int i = 0; i < 5; ++i) xw[i] = ((const unsigned*)p.x)[lane + 64 * i];
  const int tt = t < 480 ? t : 479;
  const int rg = tt / 30, ch = tt % 30;
  unsigned xr[5];
  v4u wv[5];
  {
    const bool isq = ch < 24;
    const unsigned short* wsel = isq ? p.wq : p.w1;
    const unsigned ldw = isq ? NH * DH : FF;
    const unsigned coff = isq ? DH * h + 8 * ch : 48 * h + 8 * (ch - 24);
    const unsigned short* base = wsel + (size_t)((80 * s + rg) * ldw + coff);
#pragma unroll
    for (int i = 0; i < 5; ++i) wv[i] = __builtin_nontemporal_load((const v4u*)(base + (size_t)(i * 16 * ldw)));
#pragma unroll
    for (int i = 0; i < 5; ++i) xr[i] = ((const unsigned*)p.x)[(80 * s + rg + 16 * i) >> 1];
  }
  unsigned zero_v;
  asm volatile("v_mov_b32 %0, 0" : "=v"(zero_v));
  v2u Ev = __builtin_amdgcn_raw_buffer_load_b64(rws, WS_EPOCH + zero_v, 0, 16);
  const int hr0 = 12 * rq + w, hr1 = 12 * rq + 8 + (w & 3);
  if (t == 0) cnt[0] = 0u;
  FENCE();
  __builtin_amdgcn_s_barrier();
  FENCE();
  ISSUE(1, ubase);
  asm volatile("" : "+v"(Ev), "+v"(xw[0]), "+v"(xw[1]), "+v"(xw[2]), "+v"(xw[3]), "+v"(xw[4]) :: "memory");
  __builtin_amdgcn_sched_barrier(0);
  Tag tg;
  u64 Enew;
  {
    const u64 E = ((u64)__builtin_amdgcn_readfirstlane(Ev.y) << 32) | (unsigned)__builtin_amdgcn_readfirstlane(Ev.x);
    u64 z = E + 0x9E3779B97F4A7C15ull;
    z = (z ^ (z >> 30)) * 0xBF58476D1CE4E5B9ull; z = (z ^ (z >> 27)) * 0x94D049BB133111EBull; z ^= z >> 31;
    Enew = z; tg.t0 = (unsigned)z << 24; tg.t1 = (unsigned)(z >> 8) | 1u;
  }
  {
    float sum = 0.f;
#pragma unroll
    for (int i = 0; i < 5; ++i) sum += lo_f(xw[i]) + hi_f(xw[i]);
    const float mean = wave_sum(sum) * (1.f / DM);
    float var = 0.f;
#pragma unroll
    for (int i = 0; i < 5; ++i) { const float a = lo_f(xw[i]) - mean, b = hi_f(xw[i]) - mean; var += a * a + b * b; }
    var = wave_sum(var) * (1.f / DM);
    const float mb = rbf(mean), den = rbf(sqrtf(rbf(rbf(var) + 1e-5f)));
    float a8[8] = {0.f, 0.f, 0.f, 0.f, 0.f, 0.f, 0.f, 0.f};
#pragma unroll
    for (int i = 0; i < 5; ++i) {
      const float xe = (rg & 1) ? hi_f(xr[i]) : lo_f(xr[i]);
      const float xn = rbf(rbf(xe - mb) / den);
#pragma unroll
      for (int e = 0; e < 4; ++e) { a8[2 * e] += xn * lo_f(wv[i][e]); a8[2 * e + 1] += xn * hi_f(wv[i][e]); }
    }
    if (t < 480) {
      float4* dst = (float4*)(red + rg * 240 + ch * 8);
      dst[0] = make_float4(a8[0], a8[1], a8[2], a8[3]); dst[1] = make_float4(a8[4], a8[5], a8[6], a8[7]);
    }
  }
  wg_barrier();
  if (t < 120) {
    v2f r = v2f{0.f, 0.f};
#pragma unroll
    for (int i = 0; i < 16; ++i) r += *(const v2f*)(red + i * 240 + 2 * t);
    st2<4>(rws, WS_QX + (unsigned)bid * 1920u + (unsigned)t * 16u, r.x, r.y, tg);
  }
  FENCE();
  __builtin_amdgcn_s_barrier();
  FENCE();
  const unsigned gq_off = WS_QX + (unsigned)(h * 8 + (lane >> 3)) * 1920u + (unsigned)w * 256u + (unsigned)(lane & 7) * 16u;
  const unsigned gq1 = w < 7 ? 128u : 0u;
#define GQ_LOAD(G) do { G[0] = ld2(rws, gq_off); G[1] = ld2(rws, gq_off + gq1); FENCE(); } while (0)
#define GQ_OK(G) __all((mism<4>(G[0], tg) | mism<4>(G[1], tg)) == 0u)
#define GQ_DEC(G) do { qd[0] = dec_a<4>(G[0]); qd[1] = dec_b<4>(G[0]); qd[2] = dec_a<4>(G[1]); qd[3] = dec_b<4>(G[1]); } while (0)
  v4u gqa[2];
  float qd[4];
  ISSUE(2, ubase);
  const int l40 = lane < 40 ? lane : 39;
  v4u w2_r[2];
  w2_r[0] = __builtin_nontemporal_load((const v4u*)(p.w2 + (size_t)(48 * h + hr0) * DM + 320 * chf + 8 * l40));
  w2_r[1] = __builtin_nontemporal_load((const v4u*)(p.w2 + (size_t)(48 * h + hr1) * DM + 320 * chf + 8 * l40));
  const unsigned b1a = ((const unsigned*)p.b1)[((48 * h + hr0) >> 1) + zero_v], b1b = ((const unsigned*)p.b1)[((48 * h + hr1) >> 1) + zero_v];
  FENCE();
  __builtin_amdgcn_s_barrier();
  FENCE();
  GQ_LOAD(gqa);
  __builtin_amdgcn_s_barrier();
  FENCE();
  ISSUE(3, ubase);
  GQ_DEC(gqa);
  if (!GQ_OK(gqa)) {
#pragma unroll 1
    for (unsigned spin = 0; spin < SPIN_CAP; ++spin) {
      v4u gqb[2];
      GQ_LOAD(gqb);
      GQ_DEC(gqb);
      if (GQ_OK(gqb)) break;
      __builtin_amdgcn_s_sleep(4);
    }
  }
  FENCE();
  {
#pragma unroll
    for (int i = 0; i < 4; ++i) qd[i] = grp8_sum(qd[i]);
    if (lane < 8) {
#pragma unroll
      for (int i = 0; i < 2; ++i) {
        const int col = 32 * w + 16 * i + 2 * lane;
        if (col < 192) qsh[col >> 1] = (unsigned)f2bf(qd[2 * i]) | ((unsigned)f2bf(qd[2 * i + 1]) << 16);
        else if (col < 240) *(v2f*)(hid + col - 192) = v2f{qd[2 * i], qd[2 * i + 1]};
      }
    }
  }
  wg_barrier();
  v4u qv[3];
#pragma unroll
  for (int j = 0; j < 3; ++j) qv[j] = *(const v4u*)(qsh + j * 32 + (lane & 7) * 4);
  const float hraw0 = hid[hr0], hraw1 = hid[hr1];
  {
    float hv[2];
#pragma unroll
    for (int i = 0; i < 2; ++i) {
      const int hr = i ? hr1 : hr0;
      const unsigned bw = i ? b1b : b1a;
      const float hh = rbf(rbf(i ? hraw1 : hraw0) + ((hr & 1) ? hi_f(bw) : lo_f(bw)));
      const float u2 = 2.f * 0.7978845608028654f * (hh + 0.044715f * hh * hh * hh);
      hv[i] = rbf(hh / (1.f + __expf(-u2)));
    }
    if (w >= 4) hv[1] = 0.f;
    float y[8];
#pragma unroll
    for (int e = 0; e < 4; ++e) {
      y[2 * e] = hv[0] * lo_f(w2_r[0][e]) + hv[1] * lo_f(w2_r[1][e]);
      y[2 * e + 1] = hv[0] * hi_f(w2_r[0][e]) + hv[1] * hi_f(w2_r[1][e]);
    }
    if (lane < 40) {
      float4* dst = (float4*)(ysh + w * 320 + 8 * lane);
      dst[0] = make_float4(y[0], y[1], y[2], y[3]); dst[1] = make_float4(y[4], y[5], y[6], y[7]);
    }
  }
  FENCE();

  v2f acc[12];
#pragma unroll
  for (int i = 0; i < 12; ++i) acc[i] = v2f{0.f, 0.f};
  float m = -1e30f, l = 0.f;
#define CONSUME(slot) do { \
    float d0 = 0.f, d1 = 0.f, d2 = 0.f; \
    _Pragma("unroll") for (int e = 0; e < 4; ++e) { d0 = dot2(kb[slot][0][e], qv[0][e], d0); d1 = dot2(kb[slot][1][e], qv[1][e], d1); d2 = dot2(kb[slot][2][e], qv[2][e], d2); } \
    float dsum = (d0 + d1) + d2; \
    dsum += dpp<0xB1>(dsum); dsum += dpp<0x4E>(dsum); dsum += dpp<0x141>(dsum); \
    const float tt_ = rbf(rbf(dsum) * SCALE) * LOG2E; \
    const float mn = fmaxf(m, tt_); \
    const float al = __builtin_amdgcn_exp2f(m - mn), pp = __builtin_amdgcn_exp2f(tt_ - mn); \
    l = l * al + pp; m = mn; \
    const v2f al2 = v2f{al, al}, pp2 = v2f{pp, pp}; \
    _Pragma("unroll") for (int jj = 0; jj < 3; ++jj) _Pragma("unroll") for (int e = 0; e < 4; ++e) { \
      const unsigned u_ = vb[slot][jj][e]; \
      acc[jj * 4 + e] = __builtin_elementwise_fma(pp2, v2f{lo_f(u_), hi_f(u_)}, acc[jj * 4 + e] * al2); } \
    asm volatile("" : "+v"(acc[0]), "+v"(acc[1]), "+v"(acc[2]), "+v"(acc[3]), "+v"(acc[4]), "+v"(acc[5]), "+v"(acc[6]), "+v"(acc[7]), \
                      "+v"(acc[8]), "+v"(acc[9]), "+v"(acc[10]), "+v"(acc[11]), "+v"(m), "+v"(l) :: "memory"); \
    __builtin_amdgcn_sched_barrier(0); } while (0)
#define WAVE_MERGE(buf) do { \
    const float M_ = grp8_max(m); \
    const float sc_ = __builtin_amdgcn_exp2f(m - M_); \
    const float l_ = grp8_sum(l * sc_); \
    float a_[24], b_[12], c_[6]; \
    _Pragma("unroll") for (int i = 0; i < 12; ++i) { a_[2 * i] = acc[i].x * sc_; a_[2 * i + 1] = acc[i].y * sc_; } \
    _Pragma("unroll") for (int i = 0; i < 12; ++i) b_[i] = sw32(a_[i], a_[i + 12]); \
    _Pragma("unroll") for (int i = 0; i < 6; ++i) c_[i] = sw16(b_[i], b_[i + 6]); \
    _Pragma("unroll") for (int i = 0; i < 6; ++i) c_[i] += dpp<0x128>(c_[i]); \
    float* mb_ = mrg + (buf) * 1600 + w * 200; \
    if ((lane & 8) == 0) { v2f* dst_ = (v2f*)(mb_ + ((lane >> 4) * 8 + (lane & 7)) * 6); \
      dst_[0] = v2f{c_[0], c_[1]}; dst_[1] = v2f{c_[2], c_[3]}; dst_[2] = v2f{c_[4], c_[5]}; } \
    if (lane == 0) *(v2f*)(mb_ + 192) = v2f{M_, l_}; } while (0)
  const int pa = 8 * (t >> 5) + 2 * (t & 3), pc8 = (t >> 2) & 7;
  const int ppos = t < 96 ? ((pa / 6) * 8 + pc8) * 6 + pa % 6 : 192;
#define PUBLISH(active, buf, rec) do { \
    v2f r_ = v2f{0.f, 0.f}; unsigned off_ = OOR; \
    if ((active) && t < 97) { \
      const float* mb_ = mrg + (buf) * 1600; \
      float mw_[NW]; float MM_ = -1e30f; \
      _Pragma("unroll") for (int i = 0; i < NW; ++i) { mw_[i] = mb_[i * 200 + 192]; MM_ = fmaxf(MM_, mw_[i]); } \
      MM_ = q24(MM_);     \
      _Pragma("unroll") for (int i = 0; i < NW; ++i) { const float e_ = __builtin_amdgcn_exp2f(mw_[i] - MM_); r_ += v2f{e_, e_} * *(const v2f*)(mb_ + i * 200 + ppos); } \
      if (t == 96) r_.x = MM_; \
      off_ = t < 96 ? WS_PART + (unsigned)(h * NREC + (rec)) * 1536u + (unsigned)t * 16u : WS_CTL + (unsigned)h * CTL_STR + (unsigned)(rec) * 16u; } \
    st2<8>(rws, off_, r_.x, r_.y, tg); FENCE(); } while (0)

  const __amdgpu_buffer_rsrc_t rc = __builtin_amdgcn_make_buffer_rsrc((void*)(p.ws + WS_CTL + (size_t)h * CTL_STR + 1280), 0, 4, 0x00020000);
  typedef __attribute__((address_space(3))) unsigned lu32;
  volatile lu32* nx = (volatile lu32*)(cnt + 2);
  unsigned nxt = (unsigned)(s + NSPLIT);
  int it = 0, rec_cur = s, rec_pend = 0;
  bool pend = false;
  while (nxt < (unsigned)NSC) {
    const bool dyn = it + 2 >= NSTAT;
    const unsigned tk = __builtin_amdgcn_raw_ptr_buffer_atomic_add_i32(1, rc, (dyn && t == 0) ? 0u : 0x100u, 0, 16);
    FENCE();
    PUBLISH(pend, (it + 1) & 1, rec_pend);
    const unsigned base = (nxt * NW + (unsigned)w) * (RING * 3072u);
#pragma unroll
    for (int d = 0; d < RING; ++d) { CONSUME(d); ISSUE(d, base); }
    pend = it >= NSTAT - 1;
    if (pend) {
      WAVE_MERGE(it & 1);
#pragma unroll
      for (int i = 0; i < 12; ++i) acc[i] = v2f{0.f, 0.f};
      m = -1e30f; l = 0.f;
      rec_pend = rec_cur;
    }
    const unsigned tks = __builtin_amdgcn_readfirstlane(tk);
    if (t == 0) nx[it & 1] = dyn ? (tks < (unsigned)(NSC - NSPLIT * NSTAT) ? tks + (unsigned)(NSPLIT * NSTAT) : (unsigned)NSC) : (unsigned)(s + NSPLIT * (it + 2));
    wg_barrier();
    FENCE();
    rec_cur = it + 1 >= NSTAT ? NSPLIT + (int)nxt - NSPLIT * NSTAT : s;
    nxt = __builtin_amdgcn_readfirstlane(nx[it & 1]);
    ++it;
  }
  PUBLISH(pend, (it + 1) & 1, rec_pend);
  const int fb = bid < 40 ? bid : 0;
  CONSUME(0);
  CONSUME(1);
  {
    unsigned c_ = 0;
    if (lane == 0) c_ = __hip_atomic_fetch_add(cnt, 1u, __ATOMIC_RELAXED, __HIP_MEMORY_SCOPE_WORKGROUP);
    c_ = __builtin_amdgcn_readfirstlane(c_);
    st2<8>(rws, (c_ == NW - 1 && lane == 0) ? WS_CTL + (unsigned)h * CTL_STR + 1024u + (unsigned)s * 16u : OOR, 0.f, 0.f, tg);
    FENCE();
  }
  const unsigned xfin = ((const unsigned*)p.x)[8 * fb + (lane & 7)], b2fin = ((const unsigned*)p.b2)[8 * fb + (lane & 7)];
  v4u wo_r[6];
#pragma unroll
  for (int i = 0; i < 6; ++i) wo_r[i] = __builtin_nontemporal_load((const v4u*)(p.wo + (size_t)(DH * h + 48 * rq + 6 * w + i) * DM + 320 * chf + 8 * l40));
  FENCE();
  CONSUME(2);
  CONSUME(3);

  WAVE_MERGE(it & 1);
  wg_barrier();
  PUBLISH(true, it & 1, rec_cur);
  if (t >= 128 && t < 288) {
    const int ty = t - 128;
    v2f r = v2f{0.f, 0.f};
#pragma unroll
    for (int i = 0; i < NW; ++i) r += *(const v2f*)(ysh + i * 320 + 2 * ty);
    st2<8>(rws, WS_SP + (unsigned)(256 + bid) * 2560u + (unsigned)ty * 16u, r.x, r.y, tg);
  }
  FENCE();
  v2f wf[6][4];
#pragma unroll
  for (int i = 0; i < 6; ++i)
#pragma unroll
    for (int e = 0; e < 4; ++e) wf[i][e] = v2f{lo_f(wo_r[i][e]), hi_f(wo_r[i][e])};
#pragma unroll
  for (int i = 0; i < 6; ++i) asm volatile("" : "+v"(wf[i][0]), "+v"(wf[i][1]), "+v"(wf[i][2]), "+v"(wf[i][3]));
  FENCE();
  if (w == NW - 1) {
    const unsigned cbase = WS_CTL + (unsigned)h * CTL_STR;
    unsigned po[NRND], mo[NRND];
    float ar[NRND][6], ms[NRND], ls[NRND];
#pragma unroll
    for (int r = 0; r < NRND; ++r) {
      po[r] = WS_PART + (unsigned)(h * NREC + 8 * r + (lane >> 3)) * 1536u + (unsigned)rq * 384u + (unsigned)(lane & 7) * 16u;
      mo[r] = cbase + (unsigned)(8 * r + (lane >> 3)) * 16u;
      ms[r] = 0.f; ls[r] = 1.f;
#pragma unroll
      for (int i = 0; i < 6; ++i) ar[r][i] = 0.f;
    }
#pragma unroll 1
    for (unsigned spin = 0; spin < SPIN_CAP; ++spin) {
      v4u g[NRND][4];
#pragma unroll
      for (int r = 0; r < NRND; ++r) { g[r][0] = ld2(rws, po[r]); g[r][1] = ld2(rws, po[r] + 128u); g[r][2] = ld2(rws, po[r] + 256u); g[r][3] = ld2(rws, mo[r]); }
      unsigned all_po = OOR;
#pragma unroll
      for (int r = 0; r < NRND; ++r) {
        const bool got = ((mism<8>(g[r][0], tg) | mism<8>(g[r][1], tg)) | (mism<8>(g[r][2], tg) | mism<8>(g[r][3], tg))) == 0u;
        if (got) {
          ar[r][0] = dec_a<8>(g[r][0]); ar[r][1] = dec_b<8>(g[r][0]); ar[r][2] = dec_a<8>(g[r][1]);
          ar[r][3] = dec_b<8>(g[r][1]); ar[r][4] = dec_a<8>(g[r][2]); ar[r][5] = dec_b<8>(g[r][2]);
          ms[r] = dec_a<8>(g[r][3]); ls[r] = dec_b<8>(g[r][3]); po[r] = OOR; mo[r] = OOR;
        }
        all_po &= po[r];
      }
      if (__all(all_po == OOR)) break;
      if (spin == 0) {
#pragma unroll 1
        for (unsigned sp2 = 0; sp2 < SPIN_CAP; ++sp2) {
          const v4u gh = ld2(rws, cbase + 1024u + (unsigned)(lane & 7) * 16u);
          if (__all(mism<8>(gh, tg) == 0u)) break;
          __builtin_amdgcn_s_sleep(HINT_SLEEP);
        }
      } else {
        __builtin_amdgcn_s_sleep(POLL_SLEEP);
      }
    }
    float MM = ms[0];
#pragma unroll
    for (int r = 1; r < NRND; ++r) MM = fmaxf(MM, ms[r]);
    MM = grp8_max(MM);
    float er[NRND], Ls = 0.f;
#pragma unroll
    for (int r = 0; r < NRND; ++r) { er[r] = __builtin_amdgcn_exp2f(ms[r] - MM); Ls += er[r] * ls[r]; }
    const float inv = 1.f / grp8_sum(Ls);
    float av[6];
#pragma unroll
    for (int i = 0; i < 6; ++i) {
      float v = 0.f;
#pragma unroll
      for (int r = 0; r < NRND; ++r) v += er[r] * ar[r][i];
      av[i] = rbf(grp8_sum(v) * inv);
    }
    if (lane < 8) {
#pragma unroll
      for (int i = 0; i < 3; ++i) *(v2f*)(osh + 16 * i + 2 * lane) = v2f{av[2 * i], av[2 * i + 1]};
    }
  }
  FENCE();
  wg_barrier();
  {
    const v2f o01 = *(const v2f*)(osh + 6 * w), o23 = *(const v2f*)(osh + 6 * w + 2), o45 = *(const v2f*)(osh + 6 * w + 4);
    v2f a[4];
#pragma unroll
    for (int e = 0; e < 4; ++e) {
      const v2f u0 = __builtin_elementwise_fma(v2f{o01.y, o01.y}, wf[1][e], v2f{o01.x, o01.x} * wf[0][e]);
      const v2f u1 = __builtin_elementwise_fma(v2f{o23.y, o23.y}, wf[3][e], v2f{o23.x, o23.x} * wf[2][e]);
      const v2f u2 = __builtin_elementwise_fma(v2f{o45.y, o45.y}, wf[5][e], v2f{o45.x, o45.x} * wf[4][e]);
      a[e] = (u0 + u1) + u2;
    }
    if (lane < 40) {
      float4* dst = (float4*)(ssh + w * 320 + 8 * lane);
      dst[0] = make_float4(a[0].x, a[0].y, a[1].x, a[1].y); dst[1] = make_float4(a[2].x, a[2].y, a[3].x, a[3].y);
    }
  }
  wg_barrier();
  if (t < 160) {
    v2f r = v2f{0.f, 0.f};
#pragma unroll
    for (int i = 0; i < NW; ++i) r += *(const v2f*)(ssh + i * 320 + 2 * t);
    st2<8>(rws, WS_SP + (unsigned)bid * 2560u + (unsigned)t * 16u, r.x, r.y, tg);
  }
  if (bid < 40) {
    const int fch = fb >= 20 ? 1 : 0;
    const unsigned coff = (unsigned)(16 * fb - 320 * fch) * 8u + (unsigned)(lane & 7) * 16u;
    unsigned so[4];
#pragma unroll
    for (int i = 0; i < 4; ++i) so[i] = WS_SP + (unsigned)((i >> 1) * 256 + 2 * (16 * w + 8 * (i & 1) + (lane >> 3)) + fch) * 2560u + coff;
    float va[4] = {0.f, 0.f, 0.f, 0.f}, vb2[4] = {0.f, 0.f, 0.f, 0.f};
#pragma unroll 1
    for (unsigned spin = 0; spin < SPIN_CAP; ++spin) {
      v4u g[4];
#pragma unroll
      for (int i = 0; i < 4; ++i) g[i] = ld2(rws, so[i]);
#pragma unroll
      for (int i = 0; i < 4; ++i) if (mism<8>(g[i], tg) == 0u) { va[i] = dec_a<8>(g[i]); vb2[i] = dec_b<8>(g[i]); so[i] = OOR; }
      if (__all(((so[0] & so[1]) & (so[2] & so[3])) == OOR)) break;
      if (__all((so[2] & so[3]) == OOR)) __builtin_amdgcn_s_sleep(POLL_SLEEP); else __builtin_amdgcn_s_sleep(FIN_SLEEP);
    }
    const float s0 = grp8_sum(va[0] + va[1]), s1 = grp8_sum(vb2[0] + vb2[1]);
    const float y0 = grp8_sum(va[2] + va[3]), y1 = grp8_sum(vb2[2] + vb2[3]);
    if (lane < 8) *(float4*)(fin + w * 32 + 4 * lane) = make_float4(s0, s1, y0, y1);
    wg_barrier();
    if (t < 8) {
      float4 r = make_float4(0.f, 0.f, 0.f, 0.f);
#pragma unroll
      for (int i = 0; i < NW; ++i) { const float4 v = *(const float4*)(fin + i * 32 + 4 * t); r.x += v.x; r.y += v.y; r.z += v.z; r.w += v.w; }
      const float ya = rbf(rbf(r.z) + lo_f(b2fin)), yb = rbf(rbf(r.w) + hi_f(b2fin));
      const float ga = rbf(ya / (1.f + __expf(-ya))), gb = rbf(yb / (1.f + __expf(-yb)));
      const unsigned res = (unsigned)f2bf(lo_f(xfin) + rbf(rbf(r.x) * ga)) | ((unsigned)f2bf(hi_f(xfin) + rbf(rbf(r.y) * gb)) << 16);
      ((unsigned*)p.out)[8 * fb + t] = res;
      if (bid == 0 && t == 0) __hip_atomic_store((u64*)(p.ws + WS_EPOCH), Enew, __ATOMIC_RELAXED, __HIP_MEMORY_SCOPE_AGENT);
    }
  }
}

extern "C" void kernel_launch(void* const* d_in, const int* in_sizes, int n_in, void* d_out, int out_size, void* d_ws, size_t ws_size, hipStream_t stream) {
  Params p;
  p.x = (const unsigned short*)d_in[0];
  p.K = (const unsigned char*)d_in[1];
  p.V = (const unsigned char*)d_in[2];
  p.wq = (const unsigned short*)d_in[3];
  p.wo = (const unsigned short*)d_in[4];
  p.w1 = (const unsigned short*)d_in[6];
  p.b1 = (const unsigned short*)d_in[7];
  p.w2 = (const unsigned short*)d_in[8];
  p.b2 = (const unsigned short*)d_in[9];
  p.ws = (unsigned char*)d_ws;
  p.out = (unsigned short*)d_out;
  hipLaunchKernelGGL(fused_kernel, dim3(256), dim3(NT), 0, stream, p);
}
```
